```python
import torch

def _softmax(x, axis=-1):
    return torch.softmax(x, dim=axis)
def _gelu(x):
    return torch.nn.functional.gelu(x, approximate="tanh")
def _silu(x):
    return torch.nn.functional.silu(x)
def _sigmoid(x):
    return torch.sigmoid(x)
def _layernorm(x, axis=-1, eps=1e-5):
    m = x.mean(dim=axis, keepdim=True)
    v = x.var(dim=axis, keepdim=True, unbiased=False)
    return (x - m) / torch.sqrt(v + eps)
def _scatter_add(tgt, idx, src, axis=0):
    return tgt.clone().index_add_(axis, idx, src)
def _segment_sum(src, seg, n):
    out = torch.zeros((n,) + src.shape[1:], dtype=src.dtype)
    return out.index_add_(0, seg, src)
def _pad(x, pw):
    flat = tuple(v for pair in reversed(pw) for v in pair)
    return torch.nn.functional.pad(x, flat)

kernel_name = 'concat_heads__mha_block__spectral_fnet__prompt'

def setup_inputs():

    _w = (lambda a: (lambda t: t.to(torch.bfloat16) if t.is_floating_point() else t)(torch.as_tensor(a)))
    def _A_setup():
        B, S, D, H = 1, 2048, 1024, 32
        return {
            "x": _w((lambda *s: torch.randn(*s, dtype=torch.bfloat16) * 0.2)(B, S, D)),
            "wqkv": _w((lambda *s: torch.randn(*s, dtype=torch.bfloat16) * 0.8)(D, 3 * D)),
            "wo": _w((lambda *s: torch.randn(*s, dtype=torch.bfloat16) * 0.2)(D, D)),
        }
    _A_in = _A_setup()

    def _B_setup():
        B, S, D = 1, 2048, 1024
        ns = torch.arange(S)[:, None] * torch.arange(S)[None, :]
        nd = torch.arange(D)[:, None] * torch.arange(D)[None, :]
        Ws_re = torch.cos(-2.0 * torch.pi * ns / S).to(float)
        Ws_im = torch.sin(-2.0 * torch.pi * ns / S).to(float)
        Wd_re = torch.cos(-2.0 * torch.pi * nd / D).to(float)
        Wd_im = torch.sin(-2.0 * torch.pi * nd / D).to(float)
        return {
            "x": _w((lambda *s: torch.randn(*s, dtype=torch.bfloat16) * 0.05)(B, S, D)),
            "dft_seq_re": _w(Ws_re),
            "dft_seq_im": _w(Ws_im),
            "dft_hid_re": _w(Wd_re),
            "dft_hid_im": _w(Wd_im),
        }
    _B_in = _B_setup()

    return {
        "A_x": _A_in["x"],
        "A_wqkv": _A_in["wqkv"],
        "A_wo": _A_in["wo"],
        "B_x": _B_in["x"],
        "B_dft_seq_re": _B_in["dft_seq_re"],
        "B_dft_seq_im": _B_in["dft_seq_im"],
        "B_dft_hid_re": _B_in["dft_hid_re"],
        "B_dft_hid_im": _B_in["dft_hid_im"],
    }

def reference(A_x, A_wqkv, A_wo, B_x, B_dft_seq_re, B_dft_seq_im, B_dft_hid_re, B_dft_hid_im):
    def _A_ref(x, wqkv, wo):
        B, S, D = x.shape
        H = 32
        Dh = D // H
        qkv = (lambda x, *d: x.permute(*d))((x @ wqkv).reshape(B, S, 3, H, Dh), 2, 0, 3, 1, 4)
        q, k, v = qkv[0], qkv[1], qkv[2]
        scores = torch.einsum("bhqd,bhkd->bhqk", q, k) * (Dh ** -0.5)
        p = _softmax(scores, axis=-1)
        o = torch.einsum("bhqk,bhkd->bhqd", p, v)
        return (lambda x, *d: x.permute(*d))(o, 0, 2, 1, 3).reshape(B, S, D) @ wo

    def _B_ref(x, dft_seq_re, dft_seq_im, dft_hid_re, dft_hid_im):
        a = torch.einsum("bsd,ed->bse", x, dft_hid_re)
        b = torch.einsum("bsd,ed->bse", x, dft_hid_im)
        y = torch.einsum("ts,bse->bte", dft_seq_re, a) - torch.einsum("ts,bse->bte", dft_seq_im, b)
        return y

    a = _A_ref(A_x, A_wqkv, A_wo)
    b = _B_ref(B_x, B_dft_seq_re, B_dft_seq_im, B_dft_hid_re, B_dft_hid_im)
    return torch.concatenate([a, b], axis=-1)
```

```cpp
#include <hip/hip_runtime.h>
#include <hip/hip_bf16.h>
typedef unsigned short u16;
__device__ inline float bf2f(u16 b){ return __uint_as_float(((unsigned)b)<<16); }
__device__ inline u16 f2bf(float f){ unsigned u=__float_as_uint(f); u += 0x7FFFu + ((u>>16)&1u); return (u16)(u>>16); }

template<bool BT>
__global__ void gemm_naive(const u16* A, int lda, const u16* B, int ldb, const u16* A2, const u16* B2, u16* C, int ldc, int K){
  __shared__ float As[32][33], Bs[32][33];
  int tx=threadIdx.x&31, ty=threadIdx.x>>5;
  int m0=blockIdx.y*32, n0=blockIdx.x*32;
  float acc[4]={0,0,0,0};
  for(int pass=0; pass<(A2?2:1); ++pass){
    const u16* a = pass? A2:A; const u16* b = pass? B2:B; float sg = pass? -1.f:1.f;
    for(int k0=0;k0<K;k0+=32){
      for(int r=ty;r<32;r+=8){
        As[r][tx]=sg*bf2f(a[(size_t)(m0+r)*lda+k0+tx]);
        if(BT) Bs[tx][r]=bf2f(b[(size_t)(n0+r)*ldb+k0+tx]);
        else Bs[r][tx]=bf2f(b[(size_t)(k0+r)*ldb+n0+tx]);
      }
      __syncthreads();
      for(int k=0;k<32;++k){ float bv=Bs[k][tx]; for(int i=0;i<4;++i) acc[i]+=As[ty+8*i][k]*bv; }
      __syncthreads();
    }
  }
  for(int i=0;i<4;++i) C[(size_t)(m0+ty+8*i)*ldc+n0+tx]=f2bf(acc[i]);
}

__global__ void attn_naive(const u16* qkv, u16* o){
  int row=blockIdx.x, h=blockIdx.y; int lane=threadIdx.x;
  __shared__ float q[32]; __shared__ float p[2048];
  if(lane<32) q[lane]=bf2f(qkv[(size_t)row*3072+h*32+lane]);
  __syncthreads();
  float m=-1e30f;
  for(int j=lane;j<2048;j+=64){ const u16* k=qkv+(size_t)j*3072+1024+h*32; float s=0; for(int d=0;d<32;++d) s+=q[d]*bf2f(k[d]); s*=0.17677669529663687f; p[j]=s; m=fmaxf(m,s);}
  for(int o2=32;o2;o2>>=1) m=fmaxf(m,__shfl_xor(m,o2));
  float l=0; for(int j=lane;j<2048;j+=64){ float e=__expf(p[j]-m); p[j]=e; l+=e; }
  for(int o2=32;o2;o2>>=1) l+=__shfl_xor(l,o2);
  __syncthreads();
  int d=lane&31, half=lane>>5; float acc=0;
  for(int j=half;j<2048;j+=2) acc+=p[j]*bf2f(qkv[(size_t)j*3072+2048+h*32+d]);
  acc+=__shfl_xor(acc,32);
  if(lane<32) o[(size_t)row*1024+h*32+d]=f2bf(acc/l);
}

extern "C" void kernel_launch(void* const* d_in, const int* in_sizes, int n_in, void* d_out, int out_size, void* d_ws, size_t ws_size, hipStream_t stream){
  const u16* Ax=(const u16*)d_in[0]; const u16* wqkv=(const u16*)d_in[1]; const u16* wo=(const u16*)d_in[2];
  const u16* Bx=(const u16*)d_in[3]; const u16* Cr=(const u16*)d_in[4]; const u16* Ci=(const u16*)d_in[5]; const u16* Wr=(const u16*)d_in[6]; const u16* Wi=(const u16*)d_in[7];
  u16* out=(u16*)d_out; u16* ws=(u16*)d_ws;
  u16* qkv=ws; u16* o=qkv+2048*3072; u16* a=o+2048*1024; u16* b=a+2048*1024;
  gemm_naive<false><<<dim3(3072/32,2048/32),256,0,stream>>>(Ax,1024,wqkv,3072,nullptr,nullptr,qkv,3072,1024);
  gemm_naive<true><<<dim3(1024/32,2048/32),256,0,stream>>>(Bx,1024,Wr,1024,nullptr,nullptr,a,1024,1024);
  gemm_naive<true><<<dim3(1024/32,2048/32),256,0,stream>>>(Bx,1024,Wi,1024,nullptr,nullptr,b,1024,1024);
  attn_naive<<<dim3(2048,32),64,0,stream>>>(qkv,o);
  gemm_naive<false><<<dim3(1024/32,2048/32),256,0,stream>>>(o,1024,wo,1024,nullptr,nullptr,out,2048,1024);
  gemm_naive<false><<<dim3(1024/32,2048/32),256,0,stream>>>(Cr,2048,a,1024,Ci,b,out+1024,2048,2048);
}
```

```cpp
#include <hip/hip_runtime.h>
typedef unsigned short u16;
typedef __bf16 bf16x8 __attribute__((ext_vector_type(8)));
typedef __bf16 bf16x2 __attribute__((ext_vector_type(2)));
typedef short s16x4 __attribute__((ext_vector_type(4)));
typedef short s16x8 __attribute__((ext_vector_type(8)));
typedef float f32x2 __attribute__((ext_vector_type(2)));
typedef float f32x4 __attribute__((ext_vector_type(4)));
typedef float f32x16 __attribute__((ext_vector_type(16)));
typedef unsigned u32x4 __attribute__((ext_vector_type(4)));
typedef unsigned u32x2 __attribute__((ext_vector_type(2)));
#define LAS __attribute__((address_space(3)))
#define DI __device__ __forceinline__
#define SBAR() __builtin_amdgcn_sched_barrier(0)
typedef __amdgpu_buffer_rsrc_t rsrc_t;

DI rsrc_t make_rsrc(const void* p, unsigned bytes) { return __builtin_amdgcn_make_buffer_rsrc((void*)p, 0, bytes, 0x00020000); }
DI void dma16(unsigned ldsaddr, unsigned voff, rsrc_t rs, unsigned soff) {
  asm volatile("s_mov_b32 m0, %0\n\ts_nop 0\n\tbuffer_load_dwordx4 %1, %2, %3 offen lds" ::"s"(ldsaddr), "v"(voff), "s"(rs), "s"(soff) : "memory");
}
template <int N> DI void wait_vm() { asm volatile("s_waitcnt vmcnt(%0)" ::"n"(N) : "memory"); }
DI void wait_lgkm0() { asm volatile("s_waitcnt lgkmcnt(0)" ::: "memory"); }
DI void barrier() { asm volatile("s_barrier" ::: "memory"); }
DI unsigned cvtpk(float lo, float hi) { f32x2 v = {lo, hi}; bf16x2 b = __builtin_convertvector(v, bf16x2); return __builtin_bit_cast(unsigned, b); }
DI f32x16 mfma32(bf16x8 a, bf16x8 b, f32x16 c) { return __builtin_amdgcn_mfma_f32_32x32x16_bf16(a, b, c, 0, 0, 0); }
DI bf16x8 lds_rd128(unsigned addr) { return *(const LAS bf16x8*)(size_t)addr; }
DI s16x4 lds_rdtr(unsigned addr) { return __builtin_amdgcn_ds_read_tr16_b64_v4i16((LAS s16x4*)(size_t)addr); }
DI bf16x8 cat8(s16x4 a, s16x4 b) { s16x8 r = __builtin_shufflevector(a, b, 0, 1, 2, 3, 4, 5, 6, 7); return __builtin_bit_cast(bf16x8, r); }

DI int iperm(int r) { const int b = r & 3, h = (r >> 2) & 1, t = r >> 3; return b + 4 * (t & 1) + 8 * h + 16 * (t >> 1); }

DI unsigned kc_voff(int lane, int par, unsigned ldb) { return (unsigned)(lane >> 3) * ldb + (unsigned)(((lane & 7) ^ (((lane >> 4) + 4 * par) & 7)) * 16); }
DI unsigned ic_voff(int lane, unsigned ldb) { return (unsigned)(lane >> 3) * ldb + (unsigned)(((lane & 7) ^ (((lane >> 4) & 1) << 2)) * 16); }
DI unsigned kc_raddr(int lane, int row0, int ks, bool perm) { const int r = lane & 31, h = lane >> 5, row = row0 + (perm ? iperm(r) : r); return (unsigned)(row * 128 + (((2 * ks + h) ^ ((row >> 1) & 7)) * 16)); }
DI unsigned ic_raddr(int lane, int cb) {
  const int g = lane >> 4, q = (lane & 15) >> 2, pp = lane & 3, h = g >> 1, pc = 4 * (g & 1) + pp;
  const int hh = pc & 1, t = pc >> 1, ich = (t & 1) + 2 * hh + 4 * (t >> 1);
  return (unsigned)((cb >> 1) * 8192 + (8 * h + q) * 128 + (((cb & 1) ^ ((q >> 1) & 1)) * 64) + ich * 8);
}

template <int NI, int NJ> struct Frags { bf16x8 p[NI]; bf16x8 q[NJ]; };
template <int NI, int NJ, bool PIC> struct Cons {
  static constexpr int NPA = PIC ? NI : 4;
  unsigned pa[NPA]; unsigned qa[4];
  f32x16 acc[NI][NJ];
  Frags<NI, NJ> f0, f1;
  template <int KS, int IDX> DI void rd(Frags<NI, NJ>& f) {
    constexpr int seq_is_q = (IDX == 0) || (IDX == 2 && NJ > 1);
    if constexpr (seq_is_q) { constexpr int j = IDX / 2; f.q[j] = lds_rd128(qa[KS] + j * 4096); }
    else { constexpr int i = (IDX == 1) ? 0 : (NJ > 1 ? IDX - 2 : IDX - 1);
      if constexpr (PIC) { s16x4 lo = lds_rdtr(pa[i] + KS * 2048), hi = lds_rdtr(pa[i] + KS * 2048 + 512); f.p[i] = cat8(lo, hi); }
      else f.p[i] = lds_rd128(pa[KS] + i * 4096); }
  }
  template <int KS, bool RD, int M> DI void step_m(Frags<NI, NJ>& cur, Frags<NI, NJ>& nxt) {
    if constexpr (M < NI * NJ) {
      constexpr int i = M / NJ, j = M % NJ;
      acc[i][j] = mfma32(cur.p[i], cur.q[j], acc[i][j]);
      if constexpr (RD) {
        constexpr int NR = NI + NJ, NM = NI * NJ;
        constexpr int lo = 2 * M, hi = 2 * M + 2;
        if constexpr (lo < hi && lo < NR) rd<KS, lo>(nxt);
        if constexpr (lo + 1 < hi && lo + 1 < NR) rd<KS, lo + 1>(nxt);
        if constexpr (lo + 2 < hi && lo + 2 < NR) rd<KS, lo + 2>(nxt);
      }
      SBAR();
      step_m<KS, RD, M + 1>(cur, nxt);
    }
  }
  template <int KS, bool RD> DI void step(Frags<NI, NJ>& cur, Frags<NI, NJ>& nxt) { step_m<KS, RD, 0>(cur, nxt); }
  template <int KS, int G0, int M, class H> DI void steph_m(Frags<NI, NJ>& cur, Frags<NI, NJ>& nxt, H& hk) {
    if constexpr (M < NI * NJ) {
      constexpr int i = M / NJ, j = M % NJ;
      acc[i][j] = mfma32(cur.p[i], cur.q[j], acc[i][j]); SBAR();
      constexpr int NR = NI + NJ, lo = 2 * M;
      if constexpr (lo < NR) rd<KS, lo>(nxt);
      if constexpr (lo + 1 < NR) rd<KS, lo + 1>(nxt);
      hk.template at<G0 + M>();
      SBAR();
      steph_m<KS, G0, M + 1>(cur, nxt, hk);
    }
  }
  template <class H> DI void ktile_h(H& hk) {
    steph_m<0, 0, 0>(f1, f0, hk);
    steph_m<1, NI * NJ, 0>(f0, f1, hk);
    steph_m<2, 2 * NI * NJ, 0>(f1, f0, hk);
    steph_m<3, 3 * NI * NJ, 0>(f0, f1, hk);
  }
  DI void zero() {
#pragma unroll
    for (int i = 0; i < NI; ++i)
#pragma unroll
      for (int j = 0; j < NJ; ++j) acc[i][j] = f32x16{};
#pragma unroll
    for (int i = 0; i < NI; ++i) f1.p[i] = bf16x8{};
#pragma unroll
    for (int j = 0; j < NJ; ++j) f1.q[j] = bf16x8{};
  }
  DI void advance(int delta) {
#pragma unroll
    for (int i = 0; i < NPA; ++i) pa[i] += delta;
#pragma unroll
    for (int i = 0; i < 4; ++i) qa[i] += delta;
  }
  DI void ktile() {
    barrier(); SBAR();
    step<0, true>(f1, f0);
    step<1, true>(f0, f1);
    step<2, true>(f1, f0);
    step<3, true>(f0, f1);
    wait_lgkm0(); SBAR();
  }
  DI void tail() { step<0, false>(f1, f0); }
};

constexpr int K1_SLOT = 40960, K1_NS = 4;
constexpr float QSCALE = 0.17677669529663687f * 1.4426950408889634f;
struct K1Args { const u16* Ax; const u16* wqkv; const u16* Bx; const u16* Wr; const u16* Wi; u16* Q; u16* ABt; unsigned long long* dbg; };

template <int T> struct K1N { static constexpr int n = (T < 16) ? 10 : ((T < 32) ? 8 : 0); };
struct K1Prod {
  rsrc_t rx, rw, rb, rd; unsigned vk0, vk1, vic; unsigned lds0; int w; unsigned s0, n0, e0;
  template <int T> DI void issue() {
    constexpr int slot = T % K1_NS;
    const unsigned base = lds0 + slot * K1_SLOT;
    if constexpr (T < 16) {
      constexpr unsigned k0 = T * 64;
#pragma unroll
      for (int p = 0; p < 3; ++p)
#pragma unroll
        for (int g = 0; g < 2; ++g) { const unsigned kg = 2 * w + g; dma16(base + p * 8192 + kg * 1024, vic, rw, (k0 + kg * 8) * 6144u + (n0 + p * 64) * 2u); }
#pragma unroll
      for (int g = 0; g < 4; ++g) { const unsigned rg = 4 * w + g; dma16(base + 24576 + rg * 1024, (g & 1) ? vk1 : vk0, rx, (s0 + rg * 8) * 2048u + k0 * 2u); }
    } else if constexpr (T < 32) {
      constexpr unsigned k0 = (T - 16) * 64;
#pragma unroll
      for (int g = 0; g < 4; ++g) { const unsigned rg = 4 * w + g; dma16(base + rg * 1024, (g & 1) ? vk1 : vk0, rb, (s0 + rg * 8) * 2048u + k0 * 2u); }
#pragma unroll
      for (int g = 0; g < 4; ++g) { const unsigned rg = 4 * w + g; dma16(base + 16384 + rg * 1024, (g & 1) ? vk1 : vk0, rd, (e0 + rg * 8) * 2048u + k0 * 2u); }
    }
  }
  template <int T> DI void run() {
    if constexpr (T < 32) {
      wait_vm<K1N<T + 1>::n + K1N<T + 2>::n>();
      barrier();
      issue<T + 3>();
      run<T + 1>();
    }
  }
};

#define STAMP(i)
#define STAMP_END(W)
template <int MODE>
__global__ __launch_bounds__(512, 2) void k1_kernel(K1Args a) {
  extern __shared__ __attribute__((aligned(16))) char lds[];
  const int lane = threadIdx.x & 63, wid = __builtin_amdgcn_readfirstlane(threadIdx.x >> 6);
  const int xcd = blockIdx.x & 7, idx = blockIdx.x >> 3;
  const int rb = (xcd & 1) * 8 + (idx & 7), ct = (xcd >> 1) * 4 + (idx >> 3);
  const unsigned s0 = rb * 128;
  const unsigned lds0 = (unsigned)(size_t)(LAS char*)lds;
  if (wid >= 4) {
    K1Prod p; p.w = wid - 4; p.lds0 = lds0; p.s0 = s0; p.n0 = ct * 192;
    const int eg = ct * 128;
    p.e0 = eg & 1023;
    p.rx = make_rsrc(a.Ax, 2048 * 1024 * 2); p.rw = make_rsrc(a.wqkv, 1024 * 3072 * 2); p.rb = make_rsrc(a.Bx, 2048 * 1024 * 2);
    p.rd = make_rsrc(eg >= 1024 ? a.Wi : a.Wr, 1024 * 1024 * 2);
    p.vk0 = kc_voff(lane, 0, 2048); p.vk1 = kc_voff(lane, 1, 2048); p.vic = ic_voff(lane, 6144);
    STAMP(0);
    p.issue<0>(); p.issue<1>(); p.issue<2>();
    p.run<0>();
    if constexpr (MODE == 1) STAMP_END(4);
    return;
  }
  const int wi = wid & 1, wj = wid >> 1, h = lane >> 5, r = lane & 31;
  if constexpr (MODE == 1) { for (int t = 0; t < 32; ++t) barrier(); return; }
  int slot = 0;
  STAMP(1);
  {
    Cons<3, 2, true> c;
#pragma unroll
    for (int i = 0; i < 3; ++i) c.pa[i] = lds0 + ic_raddr(lane, 3 * wi + i);
#pragma unroll
    for (int k = 0; k < 4; ++k) c.qa[k] = lds0 + 24576 + kc_raddr(lane, 64 * wj, k, false);
    c.zero();
    for (int t = 0; t < 16; ++t) { c.ktile(); const int d = (slot == K1_NS - 1) ? -(K1_NS - 1) * K1_SLOT : K1_SLOT; slot = (slot + 1) & (K1_NS - 1); c.advance(d); }
    c.tail();
#pragma unroll
    for (int i = 0; i < 3; ++i) {
      const int nb = ct * 6 + 3 * wi + i;
      const float sc = nb < 32 ? QSCALE : 1.0f;
#pragma unroll
      for (int j = 0; j < 2; ++j) {
        u16* dst = a.Q + (size_t)nb * (2048 * 32) + (size_t)(s0 + 64 * wj + 32 * j + r) * 32 + 8 * h;
        const f32x16 v = c.acc[i][j] * sc;
        u32x4 w0 = {cvtpk(v[0], v[1]), cvtpk(v[2], v[3]), cvtpk(v[4], v[5]), cvtpk(v[6], v[7])};
        u32x4 w1 = {cvtpk(v[8], v[9]), cvtpk(v[10], v[11]), cvtpk(v[12], v[13]), cvtpk(v[14], v[15])};
        *(u32x4*)dst = w0; *(u32x4*)(dst + 16) = w1;
      }
    }
  }
  {
    Cons<2, 2, false> c;
#pragma unroll
    for (int k = 0; k < 4; ++k) { c.pa[k] = lds0 + kc_raddr(lane, 64 * wi, k, true); c.qa[k] = lds0 + 16384 + kc_raddr(lane, 64 * wj, k, false); }
    c.zero();
    for (int t = 0; t < 16; ++t) { c.ktile(); const int d = (slot == K1_NS - 1) ? -(K1_NS - 1) * K1_SLOT : K1_SLOT; slot = (slot + 1) & (K1_NS - 1); c.advance(d); }
    c.tail();
    const int eg = ct * 128; const float sc = eg >= 1024 ? -1.0f : 1.0f;
#pragma unroll
    for (int i = 0; i < 2; ++i)
#pragma unroll
      for (int j = 0; j < 2; ++j) {
        u16* dst = a.ABt + (size_t)(eg + 64 * wj + 32 * j + r) * 2048 + s0 + 64 * wi + 32 * i + 8 * h;
        const f32x16 v = c.acc[i][j] * sc;
        u32x4 w0 = {cvtpk(v[0], v[1]), cvtpk(v[2], v[3]), cvtpk(v[4], v[5]), cvtpk(v[6], v[7])};
        u32x4 w1 = {cvtpk(v[8], v[9]), cvtpk(v[10], v[11]), cvtpk(v[12], v[13]), cvtpk(v[14], v[15])};
        *(u32x4*)dst = w0; *(u32x4*)(dst + 16) = w1;
      }
  }
  STAMP_END(0);
}

typedef __bf16 bf16x16 __attribute__((ext_vector_type(16)));
#define PIN(x) asm volatile("" : "+v"(x))
constexpr int KV_STAGE = 8192;
DI float ex2(float x) { return __builtin_amdgcn_exp2f(x); }
DI bf16x16 cat16(u32x4 a, u32x4 b) { typedef unsigned u32x8 __attribute__((ext_vector_type(8))); u32x8 r = __builtin_shufflevector(a, b, 0, 1, 2, 3, 4, 5, 6, 7); return __builtin_bit_cast(bf16x16, r); }

DI unsigned k_voff(int lane) { return (unsigned)((lane >> 2) * 64 + (((lane & 3) ^ ((lane >> 4) & 3)) * 16)); }
DI unsigned v_voff(int lane) { return (unsigned)(lane * 16); }

struct AttnWave {
  f32x16 S[2], NM[2], O[2]; f32x4 L[2];
  u32x4 P[2][2];
  bf16x8 q[2][2], kA[2], kB[2], vA[2], vB[2];
  bf16x8 asel; int sidx;
  unsigned ka0, ka1, va;

  template <int X> DI void exp4(int i) { S[X][i] = ex2(S[X][i]); S[X][i + 1] = ex2(S[X][i + 1]); S[X][i + 2] = ex2(S[X][i + 2]); S[X][i + 3] = ex2(S[X][i + 3]); }
  template <int X> DI void cvt2(int i) { P[X][i >> 3][(i >> 1) & 3] = cvtpk(S[X][i], S[X][i + 1]); P[X][i >> 3][((i >> 1) & 3) + 1] = cvtpk(S[X][i + 2], S[X][i + 3]); }
  DI void rdK(bf16x8 (&kf)[2], int blk) { kf[0] = lds_rd128(ka0 + blk * 2048); kf[1] = lds_rd128(ka1 + blk * 2048); }
  DI void rdV(bf16x8 (&vf)[2], int blk) {
    vf[0] = cat8(lds_rdtr(va + blk * 2048), lds_rdtr(va + blk * 2048 + 512));
    vf[1] = cat8(lds_rdtr(va + blk * 2048 + 1024), lds_rdtr(va + blk * 2048 + 1536));
  }
  template <int X, int RD, bool BARIN = false> DI void slot(bf16x8 (&kf)[2], bf16x8 (&vf)[2]) {
    constexpr int Y = X ^ 1;
    S[Y] = mfma32(kf[0], q[Y][0], NM[Y]); SBAR();
    if constexpr (RD == 1 && BARIN) { asm volatile("s_waitcnt lgkmcnt(0)\n\ts_barrier" ::: "memory"); SBAR(); }
    if constexpr (RD == 1) { rdK(kA, 0); SBAR(); }
    exp4<X>(0); PIN(S[X]); SBAR();
    S[Y] = mfma32(kf[1], q[Y][1], S[Y]); SBAR();
    if constexpr (RD == 1) { rdV(vA, 0); SBAR(); }
    if constexpr (RD == 2) { rdK(kB, 1); SBAR(); }
    exp4<X>(4); cvt2<X>(0); PIN(S[X]); PIN(P[X][0]); SBAR();
    O[Y] = mfma32(vf[0], __builtin_bit_cast(bf16x8, P[Y][0]), O[Y]); SBAR();
    exp4<X>(8); cvt2<X>(4); PIN(S[X]); PIN(P[X][0]); SBAR();
    O[Y] = mfma32(vf[1], __builtin_bit_cast(bf16x8, P[Y][1]), O[Y]); SBAR();
    if constexpr (RD == 2) { rdV(vB, 1); SBAR(); }
    exp4<X>(12); cvt2<X>(8); PIN(S[X]); PIN(P[X][1]); SBAR();
    L[Y] = __builtin_amdgcn_smfmac_f32_16x16x64_bf16(asel, cat16(P[Y][0], P[Y][1]), L[Y], sidx, 0, 0); SBAR();
    cvt2<X>(12); PIN(P[X][1]); SBAR();
  }
  template <bool BARIN = false> DI void step() {
    slot<0, 1, BARIN>(kB, vB);
    slot<1, 2>(kA, vA);
    slot<0, 0>(kA, vA);
    slot<1, 0>(kB, vB);
  }
};

DI unsigned k_raddr(int lane, int s) { const int r = lane & 31, h = lane >> 5; return (unsigned)(r * 64 + (((2 * s + h) ^ ((r >> 2) & 3)) * 16)); }
DI int dchunk(int lane) { const int g = lane >> 4, pc = 4 * (g & 1) + (lane & 3), hh = pc & 1, t = pc >> 1; return (t & 1) + 2 * hh + 4 * (t >> 1); }
DI unsigned v_raddr(int lane) { const int q4 = (lane & 15) >> 2, h = lane >> 5; return (unsigned)((4 * h + q4) * 64 + dchunk(lane) * 8); }
DI float swap32_max(float m) { auto rr = __builtin_amdgcn_permlane32_swap(__float_as_uint(m), __float_as_uint(m), false, false); return __builtin_fmaxf(__uint_as_float(rr[0]), __uint_as_float(rr[1])); }
DI float swap32_sum(float m) { auto rr = __builtin_amdgcn_permlane32_swap(__float_as_uint(m), __float_as_uint(m), false, false); return __uint_as_float(rr[0]) + __uint_as_float(rr[1]); }
DI float rowmax16(const f32x16& s) { float m = s[0];
#pragma unroll
  for (int i = 1; i < 16; ++i) m = __builtin_fmaxf(m, s[i]);
  return swap32_max(m); }
constexpr float MARGIN = 64.0f;

DI void attn_slow(const u16* Kh, const u16* Vh, const bf16x8 (&q)[2], int lane, f32x16& O, float& l) {
  const int r = lane & 31, h = lane >> 5;
  float m = -INFINITY; l = 0.f; O = f32x16{};
  const int d = iperm(r);
  for (int j = 0; j < 64; ++j) {
    const u16* kp = Kh + (size_t)(j * 32 + r) * 32 + 8 * h;
    f32x16 s = mfma32(*(const bf16x8*)kp, q[0], f32x16{});
    s = mfma32(*(const bf16x8*)(kp + 16), q[1], s);
    const float mn = __builtin_fmaxf(m, rowmax16(s));
    const float f = ex2(m - mn); m = mn;
    float ps = 0.f; u32x4 p0, p1;
#pragma unroll
    for (int i = 0; i < 16; ++i) s[i] = ex2(s[i] - mn);
#pragma unroll
    for (int i = 0; i < 4; ++i) { p0[i] = cvtpk(s[2 * i], s[2 * i + 1]); p1[i] = cvtpk(s[8 + 2 * i], s[9 + 2 * i]); }
#pragma unroll
    for (int i = 0; i < 4; ++i) { ps += __uint_as_float(p0[i] << 16) + __uint_as_float(p0[i] & 0xffff0000u) + __uint_as_float(p1[i] << 16) + __uint_as_float(p1[i] & 0xffff0000u); }
    l = l * f + swap32_sum(ps);
    O = O * f;
    s16x8 v0, v1;
#pragma unroll
    for (int e = 0; e < 8; ++e) { const int key = j * 32 + 8 * (e >> 2) + 4 * h + (e & 3); v0[e] = (short)Vh[(size_t)key * 32 + d]; v1[e] = (short)Vh[(size_t)(key + 16) * 32 + d]; }
    O = mfma32(__builtin_bit_cast(bf16x8, v0), __builtin_bit_cast(bf16x8, p0), O);
    O = mfma32(__builtin_bit_cast(bf16x8, v1), __builtin_bit_cast(bf16x8, p1), O);
  }
}

struct K2Args { const u16* Q; const u16* ABt; const u16* Cr; const u16* Ci; u16* O; u16* Pp; unsigned long long* dbg; int force_slow; int mode; };
constexpr int K2_NS = 3, K2_G = 32768, K2_SLOT = K2_G + KV_STAGE;
struct K2Hook {
      unsigned lw, lk, vg0, vg1, vk, vv, sp, sq, sk, sv; rsrc_t rp, rq, rk, rv;
      template <int M> DI void at() {
        if constexpr (M < 4) dma16(lw + M * 1024, (M & 1) ? vg1 : vg0, rp, sp + M * 8 * 4096u);
        else if constexpr (M < 8) dma16(lw + 16384 + (M - 4) * 1024, (M & 1) ? vg1 : vg0, rq, sq + (M - 4) * 8 * 4096u);
        else if constexpr (M == 8) dma16(lk, vk, rk, sk);
        else if constexpr (M == 9) dma16(lk + 4096, vv, rv, sv);
      }
      DI void all() { at<0>(); at<1>(); at<2>(); at<3>(); at<4>(); at<5>(); at<6>(); at<7>(); at<8>(); at<9>(); }
};
template <int MODE>
__global__ __launch_bounds__(512, 2) void k2_kernel(K2Args a) {
  extern __shared__ __attribute__((aligned(16))) char lds[];
  const int lane = threadIdx.x & 63, wid = __builtin_amdgcn_readfirstlane(threadIdx.x >> 6);
  const int xcd = blockIdx.x & 7, idx = blockIdx.x >> 3;
  const int head = xcd * 4 + (idx >> 3), qb = idx & 7;
  const unsigned lds0 = (unsigned)(size_t)(LAS char*)lds;
  const int r = lane & 31, h = lane >> 5;
  const u16* Qh = a.Q + (size_t)head * 65536; const u16* Kh = Qh + 32 * 65536; const u16* Vh = Kh + 32 * 65536;
  STAMP(0);
  if (wid >= 4) {
    const int w = wid - 4;
    const int half = xcd & 1, t0 = (xcd >> 1) * 512 + (idx & 3) * 128, e0 = (idx >> 2) * 128;
    const rsrc_t rk = make_rsrc(Kh, 65536 * 2 + 4096), rv = make_rsrc(Vh, 65536 * 2);
    const rsrc_t rp = make_rsrc(a.ABt + (size_t)half * 1024 * 2048, 1024 * 2048 * 2), rq = make_rsrc(half ? a.Ci : a.Cr, 2048 * 2048 * 2);
    const unsigned vk = k_voff(lane), vv = v_voff(lane), vg0 = kc_voff(lane, 0, 4096), vg1 = kc_voff(lane, 1, 4096);
    const unsigned sp = (unsigned)(e0 + 32 * w) * 4096u, sq = (unsigned)(t0 + 32 * w) * 4096u;
    const unsigned lw = lds0 + w * 4096, lk = lds0 + K2_G + w * 1024;
    K2Hook hk;
    hk.vg0 = vg0; hk.vg1 = vg1; hk.vk = vk; hk.vv = vv; hk.rp = rp; hk.rq = rq; hk.rk = rk; hk.rv = rv;
    hk.lw = lw; hk.lk = lk; hk.sp = sp; hk.sq = sq; hk.sk = 32 * 64u + w * 1024u; hk.sv = w * 1024u;
    auto nextstage = [&](int dl, bool adv) { hk.lw += dl; hk.lk += dl; if (adv) { hk.sp += 128; hk.sq += 128; hk.sk += 4096; hk.sv += 4096; } };
    hk.all(); nextstage(K2_SLOT, true); hk.all(); nextstage(K2_SLOT, true);
    const int wi = w & 1, wj = w >> 1;
    Cons<2, 2, false> c;
#pragma unroll
    for (int k = 0; k < 4; ++k) { c.pa[k] = lds0 + kc_raddr(lane, 64 * wi, k, true); c.qa[k] = lds0 + 16384 + kc_raddr(lane, 64 * wj, k, false); }
    c.zero();
    int slot = 0;
    for (int t = 0; t < 32; ++t) {
      asm volatile("s_waitcnt vmcnt(10) lgkmcnt(0)\n\ts_barrier" ::: "memory"); SBAR();
      if constexpr (MODE != 1) c.ktile_h(hk); else hk.all();
      const int d = slot == K2_NS - 1 ? -(K2_NS - 1) * K2_SLOT : K2_SLOT; slot = slot == K2_NS - 1 ? 0 : slot + 1;
      c.advance(d);
      nextstage(slot == 1 ? -(K2_NS - 1) * K2_SLOT : K2_SLOT, t + 3 < 32);
    }
    c.tail();
#pragma unroll
    for (int i = 0; i < 2; ++i)
#pragma unroll
      for (int j = 0; j < 2; ++j) {
        u16* dst = a.Pp + (size_t)half * 2048 * 1024 + (size_t)(t0 + 64 * wj + 32 * j + r) * 1024 + e0 + 64 * wi + 32 * i + 8 * h;
        const f32x16 v = c.acc[i][j];
        u32x4 w0 = {cvtpk(v[0], v[1]), cvtpk(v[2], v[3]), cvtpk(v[4], v[5]), cvtpk(v[6], v[7])};
        u32x4 w1 = {cvtpk(v[8], v[9]), cvtpk(v[10], v[11]), cvtpk(v[12], v[13]), cvtpk(v[14], v[15])};
        *(u32x4*)dst = w0; *(u32x4*)(dst + 16) = w1;
      }
    asm volatile("s_waitcnt vmcnt(0)" ::: "memory");
    STAMP_END(4);
    return;
  }
  AttnWave w;
  const int q0 = qb * 256 + wid * 64;
#pragma unroll
  for (int b = 0; b < 2; ++b)
#pragma unroll
    for (int s = 0; s < 2; ++s) w.q[b][s] = *(const bf16x8*)(Qh + (size_t)(q0 + 32 * b + r) * 32 + 16 * s + 8 * h);
  w.kB[0] = *(const bf16x8*)(Kh + (size_t)r * 32 + 8 * h); w.kB[1] = *(const bf16x8*)(Kh + (size_t)r * 32 + 16 + 8 * h);
#pragma unroll
  for (int b = 0; b < 2; ++b) {
    f32x16 s = mfma32(w.kB[0], w.q[b][0], f32x16{}); s = mfma32(w.kB[1], w.q[b][1], s);
    const float nm = -(rowmax16(s) + MARGIN);
#pragma unroll
    for (int i = 0; i < 16; ++i) w.NM[b][i] = nm;
    if (b == 0) w.S[0] = s + nm;
    w.O[b] = f32x16{}; w.L[b] = f32x4{};
  }
  w.S[1] = f32x16{};
  w.P[1][0] = u32x4{}; w.P[1][1] = u32x4{}; w.P[0][0] = u32x4{}; w.P[0][1] = u32x4{};
  w.vB[0] = bf16x8{}; w.vB[1] = bf16x8{};
  { const int m = lane & 15; const __bf16 one = (__bf16)1.0f, z = (__bf16)0.0f; const bool lo = (m == 0 || m == 4), hi = (m == 8 || m == 12);
    w.asel = bf16x8{lo ? one : z, lo ? one : z, lo ? one : z, lo ? one : z, hi ? one : z, hi ? one : z, hi ? one : z, hi ? one : z};
    w.sidx = (m == 4 || m == 12) ? 0xEEEE : 0x4444; }
  w.ka0 = lds0 + K2_G + k_raddr(lane, 0); w.ka1 = lds0 + K2_G + k_raddr(lane, 1); w.va = lds0 + K2_G + 4096 + v_raddr(lane);
  if (a.mode & 1) __builtin_amdgcn_s_setprio(1);
  int slot = 0;
  for (int t = 0; t < 32; ++t) {
    if constexpr (MODE != 2) w.step<true>(); else { asm volatile("s_waitcnt lgkmcnt(0)\n\ts_barrier" ::: "memory"); }
    const int d = slot == K2_NS - 1 ? -(K2_NS - 1) * K2_SLOT : K2_SLOT; slot = slot == K2_NS - 1 ? 0 : slot + 1;
    w.ka0 += d; w.ka1 += d; w.va += d;
  }
  w.O[1] = mfma32(w.vB[0], __builtin_bit_cast(bf16x8, w.P[1][0]), w.O[1]);
  w.O[1] = mfma32(w.vB[1], __builtin_bit_cast(bf16x8, w.P[1][1]), w.O[1]);
  w.L[1] = __builtin_amdgcn_smfmac_f32_16x16x64_bf16(w.asel, cat16(w.P[1][0], w.P[1][1]), w.L[1], w.sidx, 0, 0);
#pragma unroll
  for (int b = 0; b < 2; ++b) {
    const int src = (r & 15) + (r >> 4) * 32;
    float l = __shfl(w.L[b][0], src) + __shfl(w.L[b][0], src + 16);
    float amax = 0.f;
#pragma unroll
    for (int i = 0; i < 16; ++i) amax = __builtin_fmaxf(amax, __builtin_fabsf(w.O[b][i]));
    bool nan = false;
#pragma unroll
    for (int i = 0; i < 16; ++i) nan |= !(w.O[b][i] == w.O[b][i]);
    const bool bad = !(l < 0x1p126f) || !(amax < 0x1p126f) || nan || (amax < 0x1p-100f) || a.force_slow;
    if (__any(bad)) attn_slow(Kh, Vh, w.q[b], lane, w.O[b], l);
    const float inv = 1.0f / l;
    const f32x16 v = w.O[b] * inv;
    u16* dst = a.O + (size_t)(q0 + 32 * b + r) * 1024 + head * 32 + 8 * h;
    u32x4 w0 = {cvtpk(v[0], v[1]), cvtpk(v[2], v[3]), cvtpk(v[4], v[5]), cvtpk(v[6], v[7])};
    u32x4 w1 = {cvtpk(v[8], v[9]), cvtpk(v[10], v[11]), cvtpk(v[12], v[13]), cvtpk(v[14], v[15])};
    *(u32x4*)dst = w0; *(u32x4*)(dst + 16) = w1;
  }
  STAMP_END(0);
}

struct K3Args { const u16* O; const u16* wo; const u16* Pp; u16* out; unsigned long long* dbg; };
constexpr int K3_NS = 6, K3_SLOT = 24576;
DI unsigned addbf2(unsigned x, unsigned y) { return cvtpk(__uint_as_float(x << 16) + __uint_as_float(y << 16), __uint_as_float(x & 0xffff0000u) + __uint_as_float(y & 0xffff0000u)); }
__global__ __launch_bounds__(512, 2) void k3_kernel(K3Args a) {
  extern __shared__ __attribute__((aligned(16))) char lds[];
  const int lane = threadIdx.x & 63, wid = __builtin_amdgcn_readfirstlane(threadIdx.x >> 6);
  const int xcd = blockIdx.x & 7, idx = blockIdx.x >> 3;
  const int n0 = ((xcd & 1) * 4 + (idx & 3)) * 128, s0 = ((xcd >> 1) * 8 + (idx >> 2)) * 64;
  const unsigned lds0 = (unsigned)(size_t)(LAS char*)lds;
  STAMP(0);
  if (wid >= 4) {
    const int w = wid - 4;
    const rsrc_t ro = make_rsrc(a.O, 2048 * 1024 * 2), rw = make_rsrc(a.wo, 1024 * 1024 * 2);
    const unsigned vk0 = kc_voff(lane, 0, 2048), vk1 = kc_voff(lane, 1, 2048), vic = ic_voff(lane, 2048);
    auto issue = [&](int t, int slot) {
      const unsigned base = lds0 + slot * K3_SLOT, k0 = t * 64;
#pragma unroll
      for (int p = 0; p < 2; ++p)
#pragma unroll
        for (int g = 0; g < 2; ++g) { const unsigned kg = 2 * w + g; dma16(base + p * 8192 + kg * 1024, vic, rw, (k0 + kg * 8) * 2048u + (n0 + p * 64) * 2u); }
#pragma unroll
      for (int g = 0; g < 2; ++g) { const unsigned rg = 2 * w + g; dma16(base + 16384 + rg * 1024, g ? vk1 : vk0, ro, (s0 + rg * 8) * 2048u + k0 * 2u); }
    };
#pragma unroll
    for (int t = 0; t < K3_NS - 1; ++t) issue(t, t);
    int slot = K3_NS - 1;
    for (int t = 0; t < 16; ++t) {
      if (t <= 16 - K3_NS) wait_vm<6 * (K3_NS - 2)>();
      else if (t == 12) wait_vm<18>(); else if (t == 13) wait_vm<12>(); else if (t == 14) wait_vm<6>(); else if (t == 15) wait_vm<0>(); else wait_vm<6 * (K3_NS - 2)>();
      barrier();
      if (t + K3_NS - 1 < 16) issue(t + K3_NS - 1, slot);
      slot = slot == K3_NS - 1 ? 0 : slot + 1;
    }
    return;
  }
  {
    const size_t e = (size_t)blockIdx.x * 8192 + threadIdx.x * 8;
#pragma unroll
    for (int i = 0; i < 4; ++i) {
      const size_t o = e + i * 2048;
      const u32x4 x = *(const u32x4*)(a.Pp + o), y = *(const u32x4*)(a.Pp + 2048 * 1024 + o);
      const u32x4 z = {addbf2(x[0], y[0]), addbf2(x[1], y[1]), addbf2(x[2], y[2]), addbf2(x[3], y[3])};
      *(u32x4*)(a.out + (o >> 10) * 2048 + 1024 + (o & 1023)) = z;
    }
  }
  const int wi = wid & 1, wj = wid >> 1, h = lane >> 5, r = lane & 31;
  Cons<2, 1, true> c;
#pragma unroll
  for (int i = 0; i < 2; ++i) c.pa[i] = lds0 + ic_raddr(lane, 2 * wi + i);
#pragma unroll
  for (int k = 0; k < 4; ++k) c.qa[k] = lds0 + 16384 + kc_raddr(lane, 32 * wj, k, false);
  c.zero();
  int slot = 0;
  for (int t = 0; t < 16; ++t) { c.ktile(); const int d = (slot == K3_NS - 1) ? -(K3_NS - 1) * K3_SLOT : K3_SLOT; slot = slot == K3_NS - 1 ? 0 : slot + 1; c.advance(d); }
  c.tail();
#pragma unroll
  for (int i = 0; i < 2; ++i) {
    u16* dst = a.out + (size_t)(s0 + 32 * wj + r) * 2048 + n0 + 64 * wi + 32 * i + 8 * h;
    const f32x16 v = c.acc[i][0];
    u32x4 w0 = {cvtpk(v[0], v[1]), cvtpk(v[2], v[3]), cvtpk(v[4], v[5]), cvtpk(v[6], v[7])};
    u32x4 w1 = {cvtpk(v[8], v[9]), cvtpk(v[10], v[11]), cvtpk(v[12], v[13]), cvtpk(v[14], v[15])};
    *(u32x4*)dst = w0; *(u32x4*)(dst + 16) = w1;
  }
  STAMP_END(0);
}

extern "C" void kernel_launch(void* const* d_in, const int* in_sizes, int n_in, void* d_out, int out_size, void* d_ws, size_t ws_size, hipStream_t stream) {
  const u16* Ax = (const u16*)d_in[0]; const u16* wqkv = (const u16*)d_in[1]; const u16* wo = (const u16*)d_in[2];
  const u16* Bx = (const u16*)d_in[3]; const u16* Cr = (const u16*)d_in[4]; const u16* Ci = (const u16*)d_in[5];
  const u16* Wr = (const u16*)d_in[6]; const u16* Wi = (const u16*)d_in[7];
  u16* ws = (u16*)d_ws; const size_t M = (size_t)2048 * 1024;
  u16 *Q = ws, *ABt = ws + 3 * M, *O = ws + 5 * M, *Pp = ws + 6 * M;
  (void)hipFuncSetAttribute((const void*)k1_kernel<0>, hipFuncAttributeMaxDynamicSharedMemorySize, 163840);
  (void)hipFuncSetAttribute((const void*)k2_kernel<0>, hipFuncAttributeMaxDynamicSharedMemorySize, 163840);
  (void)hipFuncSetAttribute((const void*)k3_kernel, hipFuncAttributeMaxDynamicSharedMemorySize, 163840);
  K1Args a1; a1.Ax = Ax; a1.wqkv = wqkv; a1.Bx = Bx; a1.Wr = Wr; a1.Wi = Wi; a1.Q = Q; a1.ABt = ABt; a1.dbg = nullptr;
  K2Args a2; a2.Q = Q; a2.ABt = ABt; a2.Cr = Cr; a2.Ci = Ci; a2.O = O; a2.Pp = Pp; a2.dbg = nullptr; a2.force_slow = 0; a2.mode = 0;
  K3Args a3; a3.O = O; a3.wo = wo; a3.Pp = Pp; a3.out = (u16*)d_out; a3.dbg = nullptr;
  k1_kernel<0><<<256, 512, K1_SLOT * K1_NS, stream>>>(a1);
  k2_kernel<0><<<256, 512, K2_NS * K2_SLOT, stream>>>(a2);
  k3_kernel<<<256, 512, K3_NS * K3_SLOT, stream>>>(a3);
}
```

```cpp
#include <hip/hip_runtime.h>
typedef unsigned short u16;
typedef __bf16 bf16x8 __attribute__((ext_vector_type(8)));
typedef __bf16 bf16x16 __attribute__((ext_vector_type(16)));
typedef __bf16 bf16x2 __attribute__((ext_vector_type(2)));
typedef short s16x4 __attribute__((ext_vector_type(4)));
typedef short s16x8 __attribute__((ext_vector_type(8)));
typedef float f32x2 __attribute__((ext_vector_type(2)));
typedef float f32x4 __attribute__((ext_vector_type(4)));
typedef float f32x16 __attribute__((ext_vector_type(16)));
typedef unsigned u32x4 __attribute__((ext_vector_type(4)));
typedef unsigned u32x2 __attribute__((ext_vector_type(2)));
#define LAS __attribute__((address_space(3)))
#define DI __device__ __forceinline__
#define SBAR() __builtin_amdgcn_sched_barrier(0)
#define PIN(x) asm volatile("" : "+v"(x))
typedef __amdgpu_buffer_rsrc_t rsrc_t;

DI int fresh_lane() { int l; asm volatile("v_mbcnt_lo_u32_b32 %0, -1, 0\n\tv_mbcnt_hi_u32_b32 %0, -1, %0" : "=v"(l)); return l; }
DI rsrc_t make_rsrc(const void* p, unsigned bytes) { return __builtin_amdgcn_make_buffer_rsrc((void*)p, 0, bytes, 0x00020000); }
DI void dma16(unsigned ldsaddr, unsigned voff, rsrc_t rs, unsigned soff) {
  asm volatile("s_mov_b32 m0, %0\n\ts_nop 0\n\tbuffer_load_dwordx4 %1, %2, %3 offen lds" ::"s"(ldsaddr), "v"(voff), "s"(rs), "s"(soff) : "memory");
}
DI void l2warm(unsigned voff, rsrc_t rs, unsigned soff) {
  asm volatile("buffer_load_dword v167, %0, %1, %2 offen" ::"v"(voff), "s"(rs), "s"(soff) : "memory", "v167");
}
constexpr unsigned OOB = 0x7ff00000u;
template <int N> DI void wait_vm() { asm volatile("s_waitcnt vmcnt(%0)" ::"n"(N) : "memory"); }
DI void wait_lgkm0() { asm volatile("s_waitcnt lgkmcnt(0)" ::: "memory"); }
DI void barrier() { asm volatile("s_barrier" ::: "memory"); }
DI unsigned cvtpk(float lo, float hi) { f32x2 v = {lo, hi}; bf16x2 b = __builtin_convertvector(v, bf16x2); return __builtin_bit_cast(unsigned, b); }
DI f32x16 mfma32(bf16x8 a, bf16x8 b, f32x16 c) { return __builtin_amdgcn_mfma_f32_32x32x16_bf16(a, b, c, 0, 0, 0); }
DI f32x4 mfma16(bf16x8 a, bf16x8 b, f32x4 c) { return __builtin_amdgcn_mfma_f32_16x16x32_bf16(a, b, c, 0, 0, 0); }
DI bf16x8 lds_rd128(unsigned addr) { return *(const LAS bf16x8*)(size_t)addr; }
DI u32x4 lds_rdu(unsigned addr) { return *(const LAS u32x4*)(size_t)addr; }
DI void lds_wr64(unsigned addr, u32x2 v) { *(LAS u32x2*)(size_t)addr = v; }
DI void lds_wr128(unsigned addr, u32x4 v) { *(LAS u32x4*)(size_t)addr = v; }
DI s16x4 lds_rdtr(unsigned addr) { return __builtin_amdgcn_ds_read_tr16_b64_v4i16((LAS s16x4*)(size_t)addr); }
DI bf16x8 cat8(s16x4 a, s16x4 b) { s16x8 r = __builtin_shufflevector(a, b, 0, 1, 2, 3, 4, 5, 6, 7); return __builtin_bit_cast(bf16x8, r); }

DI unsigned kc_voff(int lane, int par, unsigned ldb) { return (unsigned)(lane >> 3) * ldb + (unsigned)(((lane & 7) ^ (((lane >> 4) + 4 * par) & 7)) * 16); }
DI unsigned ic_voff(int lane, int par, unsigned ldb) { const int c = lane & 7, u = (c >> 1) ^ (((lane >> 4) & 1) << 1) ^ par; return (unsigned)(lane >> 3) * ldb + (unsigned)((u * 2 + (c & 1)) * 16); }
template <int SH> DI unsigned kc_raddr(int lane, int row0, int ks) {
  const int row = row0 + (lane & (SH - 1)), ch = SH == 16 ? 4 * ks + (lane >> 4) : 2 * ks + (lane >> 5);
  return (unsigned)(row * 128 + ((ch ^ ((row >> 1) & 7)) * 16)); }
template <int SH> DI unsigned ic_raddr(int lane, int cb) {
  const int g = lane >> 4, q = (lane & 15) >> 2, p = lane & 3;
  const int kg = SH == 16 ? g : (g >> 1), c16 = SH == 16 ? cb : 2 * cb + (g & 1);
  const int m = (((q >> 1) & 1) << 1) ^ (kg & 1);
  return (unsigned)((c16 >> 2) * 8192 + (8 * kg + q) * 128 + (((c16 & 3) ^ m) * 32) + p * 8);
}
template <int SH> struct AccT; template <> struct AccT<16> { typedef f32x4 t; }; template <> struct AccT<32> { typedef f32x16 t; };

template <int SH, int NI, int NJ, bool PIC, int SWAPI = -1> struct Cons {
  static constexpr int NKS = SH == 16 ? 2 : 4, NPA = PIC ? NI : NKS, NM = NI * NJ, BLK = SH * 128, KSO = SH == 16 ? 4096 : 2048;
  typedef typename AccT<SH>::t acc_t;
  unsigned pa[NPA], qa[NKS];
  acc_t acc[NI][NJ];
  bf16x8 p0[NI], q0[NJ], p1[NI], q1[NJ];
  static constexpr int PI = PIC ? 2 : 1, NIT = NJ + NI * PI;
  s16x4 tlo;
  template <int KS, int K> DI void rd(bf16x8 (&p)[NI], bf16x8 (&q)[NJ]) {
    if constexpr (K == 0) q[0] = lds_rd128(qa[KS]);
    else if constexpr (K > PI && K < PI + NJ) q[K - PI] = lds_rd128(qa[KS] + (K - PI) * BLK);
    else { constexpr int r = K <= PI ? K - 1 : K - NJ, i = r / PI, hf = r % PI;
      if constexpr (!PIC) p[i] = lds_rd128(pa[KS] + i * BLK);
      else if constexpr (hf == 0) tlo = lds_rdtr(pa[i] + KS * KSO);
      else p[i] = cat8(tlo, lds_rdtr(pa[i] + KS * KSO + 512)); }
  }
  template <int I, int J> DI void mma(bf16x8 a, bf16x8 b) {
    if constexpr (SH == 16) acc[I][J] = mfma16(a, b, acc[I][J]);
    else if constexpr (I == SWAPI) acc[I][J] = mfma32(b, a, acc[I][J]);
    else acc[I][J] = mfma32(a, b, acc[I][J]); }
  template <int KS, bool RD, int G0, bool FIRST, int M, class H> DI void grp(bf16x8 (&cp)[NI], bf16x8 (&cq)[NJ], bf16x8 (&np)[NI], bf16x8 (&nq)[NJ], H& hk, int delta) {
    if constexpr (M < NM) {
      mma<M / NJ, M % NJ>(cp[M / NJ], cq[M % NJ]); SBAR();
      if constexpr (FIRST && M == 0) { advance(delta); hk.sync(); SBAR(); }
      constexpr int RG = NM - 1 < NIT ? (NM > 1 ? NM - 1 : 1) : NIT;
      constexpr int lo = (M * NIT + RG - 1) / RG, hi = M + 1 < RG ? ((M + 1) * NIT + RG - 1) / RG : NIT;
      if constexpr (RD && M < RG) {
        if constexpr (lo < hi) rd<KS, lo>(np, nq);
        if constexpr (lo + 1 < hi) rd<KS, lo + 1>(np, nq);
        if constexpr (lo + 2 < hi) rd<KS, lo + 2>(np, nq);
        if constexpr (lo + 3 < hi) rd<KS, lo + 3>(np, nq);
        if constexpr (lo + 4 < hi) rd<KS, lo + 4>(np, nq);
      }
      hk.template at<G0 + M>();
      SBAR();
      grp<KS, RD, G0, FIRST, M + 1>(cp, cq, np, nq, hk, delta);
    }
  }
  DI void advance(int delta) {
#pragma unroll
    for (int i = 0; i < NPA; ++i) { pa[i] += delta; PIN(pa[i]); }
#pragma unroll
    for (int i = 0; i < NKS; ++i) { qa[i] += delta; PIN(qa[i]); }
  }
  template <int G, class H> DI void group(H& hk, int delta = 0) {
    if constexpr ((G & 1) == 0) grp<G, true, G * NM, G == 0, 0>(p1, q1, p0, q0, hk, delta);
    else grp<G, true, G * NM, false, 0>(p0, q0, p1, q1, hk, 0);
  }
  template <class H> DI void tail(H& hk) { grp<0, false, 1000, false, 0>(p1, q1, p0, q0, hk, 0); }
  template <int NT, int NS, int SLOT, class H> DI void run(H& hk, int s0) {
    int slot = s0;
    group<0>(hk, 0);
    if constexpr (NKS == 4) group<1>(hk);
    for (int t = 1; t < NT; ++t) {
      const int d = slot == NS - 1 ? -(NS - 1) * SLOT : SLOT; slot = slot == NS - 1 ? 0 : slot + 1;
      if constexpr (NKS == 4) { group<2>(hk); group<3>(hk); group<0>(hk, d); group<1>(hk); }
      else { group<1>(hk); group<0>(hk, d); }
    }
    if constexpr (NKS == 4) { group<2>(hk); group<3>(hk); } else group<1>(hk);
    tail(hk);
  }
  DI void zero() {
#pragma unroll
    for (int i = 0; i < NI; ++i)
#pragma unroll
      for (int j = 0; j < NJ; ++j) acc[i][j] = acc_t{};
#pragma unroll
    for (int i = 0; i < NI; ++i) p1[i] = bf16x8{};
#pragma unroll
    for (int j = 0; j < NJ; ++j) q1[j] = bf16x8{};
  }
};
struct PlainHook { template <int M> DI void at() {} DI void sync() { asm volatile("s_waitcnt lgkmcnt(0)\n\ts_barrier" ::: "memory"); } };
struct NoSyncHook { template <int M> DI void at() {} DI void sync() { asm volatile("s_waitcnt lgkmcnt(0)" ::: "memory"); } };

DI u32x2 pk4(float a, float b, float c, float d, float sc) { return u32x2{cvtpk(a * sc, b * sc), cvtpk(c * sc, d * sc)}; }
template <int SH, int NI, int NJ, class A> DI void store_rows64(const A (&acc)[NI][NJ], int i32, float sc, unsigned stg, int lane, char* dst, size_t ldb) {
  if constexpr (SH == 16) {
    const int j = lane & 15, g = lane >> 4;
#pragma unroll
    for (int jj = 0; jj < 4; ++jj)
#pragma unroll
      for (int ii = 0; ii < 2; ++ii) { const A& v = acc[2 * i32 + ii][jj]; const int row = 16 * jj + j;
        lds_wr64(stg + row * 64 + (((2 * ii + (g >> 1)) ^ ((row >> 1) & 3)) * 16) + (g & 1) * 8, pk4(v[0], v[1], v[2], v[3], sc)); }
  } else {
    const int j = lane & 31, h = lane >> 5;
#pragma unroll
    for (int jj = 0; jj < 2; ++jj)
#pragma unroll
      for (int t = 0; t < 4; ++t) { const A& v = acc[i32][jj]; const int row = 32 * jj + j;
        lds_wr64(stg + row * 64 + ((t ^ ((row >> 1) & 3)) * 16) + h * 8, pk4(v[4 * t], v[4 * t + 1], v[4 * t + 2], v[4 * t + 3], sc)); }
  }
#pragma unroll
  for (int m = 0; m < 4; ++m) { const int row = 16 * m + (lane >> 2), c = lane & 3;
    *(u32x4*)(dst + (size_t)row * ldb + c * 16) = lds_rdu(stg + row * 64 + ((c ^ ((row >> 1) & 3)) * 16)); }
}
template <int SH, int NI, int NJ, class A> DI void store_rows128(const A (&acc)[NI][NJ], int j32, float sc, unsigned stg, int lane, char* dst, size_t ldb) {
  if constexpr (SH == 16) {
    const int j = lane & 15, g = lane >> 4;
#pragma unroll
    for (int jj = 0; jj < 2; ++jj)
#pragma unroll
      for (int ii = 0; ii < 4; ++ii) { const A& v = acc[ii][2 * j32 + jj]; const int row = 16 * jj + j;
        lds_wr64(stg + row * 128 + (((2 * ii + (g >> 1)) ^ (row & 7)) * 16) + (g & 1) * 8, pk4(v[0], v[1], v[2], v[3], sc)); }
  } else {
    const int row = lane & 31, h = lane >> 5;
#pragma unroll
    for (int ii = 0; ii < 2; ++ii)
#pragma unroll
      for (int t = 0; t < 4; ++t) { const A& v = acc[ii][j32];
        lds_wr64(stg + row * 128 + (((4 * ii + t) ^ (row & 7)) * 16) + h * 8, pk4(v[4 * t], v[4 * t + 1], v[4 * t + 2], v[4 * t + 3], sc)); }
  }
#pragma unroll
  for (int m = 0; m < 4; ++m) { const int row = 8 * m + (lane >> 3), c = lane & 7;
    *(u32x4*)(dst + (size_t)row * ldb + c * 16) = lds_rdu(stg + row * 128 + ((c ^ (row & 7)) * 16)); }
}
#define STAMP()
#define STAMP_END(W)

DI float ex2(float x) { return __builtin_amdgcn_exp2f(x); }
DI bf16x16 cat16(u32x4 a, u32x4 b) { typedef unsigned u32x8 __attribute__((ext_vector_type(8))); u32x8 r = __builtin_shufflevector(a, b, 0, 1, 2, 3, 4, 5, 6, 7); return __builtin_bit_cast(bf16x16, r); }
DI int iperm(int r) { const int b = r & 3, h = (r >> 2) & 1, t = r >> 3; return b + 4 * (t & 1) + 8 * h + 16 * (t >> 1); }
DI unsigned k_voff(int lane) { return (unsigned)((lane >> 2) * 64 + (((lane & 3) ^ ((lane >> 4) & 3)) * 16)); }
DI unsigned v_voff(int lane) { const int c = lane & 7; return (unsigned)((lane >> 3) * 4096 + ((c & 4) | ((c & 3) ^ ((lane >> 4) & 3))) * 16); }
DI unsigned k_raddr(int lane, int s) { const int r = lane & 31, h = lane >> 5; return (unsigned)(r * 64 + (((2 * s + h) ^ ((r >> 2) & 3)) * 16)); }
DI unsigned v_raddr(int lane, int s) { const int d = iperm(lane & 31), h = lane >> 5; return (unsigned)(d * 128 + (((2 * s + h) ^ ((d >> 1) & 3)) * 16)); }
DI float swap32_max(float m) { auto rr = __builtin_amdgcn_permlane32_swap(__float_as_uint(m), __float_as_uint(m), false, false); return __builtin_fmaxf(__uint_as_float(rr[0]), __uint_as_float(rr[1])); }
DI float swap32_sum(float m) { auto rr = __builtin_amdgcn_permlane32_swap(__float_as_uint(m), __float_as_uint(m), false, false); return __uint_as_float(rr[0]) + __uint_as_float(rr[1]); }
DI float rowmax16(const f32x16& s) { float m = s[0];
#pragma unroll
  for (int i = 1; i < 16; ++i) m = __builtin_fmaxf(m, s[i]);
  return swap32_max(m); }
constexpr float MARGIN = 64.0f;

struct AttnWave {
  f32x16 S[2], NM[2], O[2]; f32x4 L[2];
  u32x4 P[2][2];
  bf16x8 q[2][2], kf[2], vf[2];
  bf16x8 asel; int sidx;
  unsigned ka0, ka1, va0, va1;
  template <int X> DI void exp4(int i) { S[X][i] = ex2(S[X][i]); S[X][i + 1] = ex2(S[X][i + 1]); S[X][i + 2] = ex2(S[X][i + 2]); S[X][i + 3] = ex2(S[X][i + 3]); }
  template <int X> DI void cvt2(int i) { P[X][i >> 3][(i >> 1) & 3] = cvtpk(S[X][i], S[X][i + 1]); P[X][i >> 3][((i >> 1) & 3) + 1] = cvtpk(S[X][i + 2], S[X][i + 3]); }
  template <int X, int RD> DI void slot(int delta = 0) {
    constexpr int Y = X ^ 1, B = RD - 1;
    S[Y] = mfma32(kf[0], q[Y][0], NM[Y]); SBAR();
    if constexpr (RD == 1) { ka0 += delta; ka1 += delta; va0 += delta; va1 += delta; PIN(ka0); PIN(ka1); PIN(va0); PIN(va1); asm volatile("s_waitcnt lgkmcnt(0)\n\ts_barrier" ::: "memory"); SBAR(); }
    if constexpr (RD) { kf[0] = lds_rd128(ka0 + B * 2048); SBAR(); }
    exp4<X>(0); PIN(S[X]); SBAR();
    S[Y] = mfma32(kf[1], q[Y][1], S[Y]); SBAR();
    if constexpr (RD) { kf[1] = lds_rd128(ka1 + B * 2048); SBAR(); }
    exp4<X>(4); cvt2<X>(0); PIN(S[X]); PIN(P[X][0]); SBAR();
    O[Y] = mfma32(vf[0], __builtin_bit_cast(bf16x8, P[Y][0]), O[Y]); SBAR();
    if constexpr (RD) { vf[0] = lds_rd128(va0 + B * 64); SBAR(); }
    exp4<X>(8); cvt2<X>(4); PIN(S[X]); PIN(P[X][0]); SBAR();
    O[Y] = mfma32(vf[1], __builtin_bit_cast(bf16x8, P[Y][1]), O[Y]); SBAR();
    if constexpr (RD) { vf[1] = lds_rd128(va1 + B * 64); SBAR(); }
    exp4<X>(12); cvt2<X>(8); PIN(S[X]); PIN(P[X][1]); SBAR();
    L[Y] = __builtin_amdgcn_smfmac_f32_16x16x64_bf16(asel, cat16(P[Y][0], P[Y][1]), L[Y], sidx, 0, 0); SBAR();
    cvt2<X>(12); PIN(P[X][1]); SBAR();
  }
  template <int NS, int SLOT> DI void run() {
    slot<0, 1>(0); slot<1, 0>();
    int sl = 0;
    for (int t = 1; t < 32; ++t) {
      const int d = sl == NS - 1 ? -(NS - 1) * SLOT : SLOT; sl = sl == NS - 1 ? 0 : sl + 1;
      slot<0, 2>(); slot<1, 0>(); slot<0, 1>(d); slot<1, 0>();
    }
    slot<0, 2>(); slot<1, 0>();
    O[1] = mfma32(vf[0], __builtin_bit_cast(bf16x8, P[1][0]), O[1]);
    O[1] = mfma32(vf[1], __builtin_bit_cast(bf16x8, P[1][1]), O[1]);
    L[1] = __builtin_amdgcn_smfmac_f32_16x16x64_bf16(asel, cat16(P[1][0], P[1][1]), L[1], sidx, 0, 0);
  }
};

DI void attn_slow(const u16* Kh, const u16* Vt, const bf16x8 (&q)[2], int lane, f32x16& O, float& l) {
  const int r = lane & 31, h = lane >> 5;
  float m = -INFINITY; l = 0.f; O = f32x16{};
  const u16* vrow = Vt + (size_t)iperm(r) * 2048 + 8 * h;
  for (int j = 0; j < 64; ++j) {
    const u16* kp = Kh + (size_t)(j * 32 + r) * 32 + 8 * h;
    f32x16 s = mfma32(*(const bf16x8*)kp, q[0], f32x16{});
    s = mfma32(*(const bf16x8*)(kp + 16), q[1], s);
    const float mn = __builtin_fmaxf(m, rowmax16(s));
    const float f = ex2(m - mn); m = mn;
    float ps = 0.f; u32x4 p0, p1;
#pragma unroll
    for (int i = 0; i < 16; ++i) s[i] = ex2(s[i] - mn);
#pragma unroll
    for (int i = 0; i < 4; ++i) { p0[i] = cvtpk(s[2 * i], s[2 * i + 1]); p1[i] = cvtpk(s[8 + 2 * i], s[9 + 2 * i]); }
#pragma unroll
    for (int i = 0; i < 4; ++i) { ps += __uint_as_float(p0[i] << 16) + __uint_as_float(p0[i] & 0xffff0000u) + __uint_as_float(p1[i] << 16) + __uint_as_float(p1[i] & 0xffff0000u); }
    l = l * f + swap32_sum(ps);
    O = O * f;
    O = mfma32(*(const bf16x8*)(vrow + j * 32), __builtin_bit_cast(bf16x8, p0), O);
    O = mfma32(*(const bf16x8*)(vrow + j * 32 + 16), __builtin_bit_cast(bf16x8, p1), O);
  }
}

#include <type_traits>
constexpr float QSCALE = 0.17677669529663687f * 1.4426950408889634f;
constexpr int SLOT40 = 40960;
struct Args {
  const u16 *Ax, *wqkv, *wo, *Bx, *Cr, *Ci, *Wr, *Wi;
  u16 *Q, *ABt, *O, *Pp, *out;
  unsigned long long* dbg; int force_slow; int mode;
};
#define T2_SH 32
#define G_SH 16
#define T5_SH 32
#define PF_ON 1
#define TLB(off, t)
template <int N> using IC = std::integral_constant<int, N>;
struct Ids { int lane, wid, xcd, idx; unsigned lds0; };

DI void p1_prod(const Args& a, const Ids& id) {
  const int lane = id.lane, w = id.wid - 8, rl = id.idx & 7, cl = id.idx >> 3;
  const unsigned s0 = ((id.xcd & 1) * 8 + rl) * 128, ct = (id.xcd >> 1) * 4 + cl, eg = ct * 128, e0 = eg & 1023;
  const rsrc_t rx = make_rsrc(a.Ax, 2048 * 1024 * 2), rw = make_rsrc(a.wqkv, 1024 * 3072 * 2), rb = make_rsrc(a.Bx, 2048 * 1024 * 2);
  const rsrc_t rd = make_rsrc(eg >= 1024 ? a.Wi : a.Wr, 1024 * 1024 * 2);
  const unsigned vk0 = kc_voff(lane, 0, 2048), vk1 = kc_voff(lane, 1, 2048), vi0 = ic_voff(lane, 0, 6144), vi1 = ic_voff(lane, 1, 6144);
  const rsrc_t pr1 = w < 2 ? rx : rw, pr2 = w < 2 ? rb : rd;
  const int wl = (w & 1) * 64 + lane;
  const unsigned pvx = (s0 + 32 * cl + (lane & 31)) * 2048u + ((lane >> 5) + 2 * (w & 1)) * 128u;
  const unsigned pv1 = w < 2 ? pvx : (wl < 96 ? (32 * rl + wl / 3) * 6144u + (wl % 3) * 2048u + ct * 128u : OOB);
  const unsigned pv2 = w < 2 ? pvx : (w == 2 ? (e0 + 16 * rl + (lane & 15)) * 2048u + (lane >> 4) * 128u : OOB);
  const unsigned ps1 = w < 2 ? 128u : 64u * 6144u;
  auto warm = [&](int ts) {
    if (!PF_ON) { l2warm(OOB, pr1, 0); return; }
    if (ts < 16) l2warm(pv1, pr1, ts * ps1); else l2warm(ts < 32 ? pv2 : OOB, pr2, (ts - 16) * 128u); };
  auto issue = [&](int ts) {
    const unsigned base = id.lds0 + (ts & 3) * SLOT40;
    if (ts < 16) {
      const unsigned k0 = ts * 64;
#pragma unroll
      for (int p = 0; p < 3; ++p)
#pragma unroll
        for (int g = 0; g < 2; ++g) { const unsigned kg = 2 * w + g; dma16(base + p * 8192 + kg * 1024, g ? vi1 : vi0, rw, (k0 + kg * 8) * 6144u + p * 2048u + ct * 128u); }
#pragma unroll
      for (int g = 0; g < 4; ++g) { const unsigned rg = 4 * w + g; dma16(base + 24576 + rg * 1024, (g & 1) ? vk1 : vk0, rx, (s0 + rg * 8) * 2048u + k0 * 2u); }
    } else if (ts < 32) {
      const unsigned k0 = (ts - 16) * 64;
#pragma unroll
      for (int g = 0; g < 4; ++g) { const unsigned rg = 4 * w + g; dma16(base + rg * 1024, (g & 1) ? vk1 : vk0, rb, (s0 + rg * 8) * 2048u + k0 * 2u); }
#pragma unroll
      for (int g = 0; g < 4; ++g) { const unsigned rg = 4 * w + g; dma16(base + 16384 + rg * 1024, (g & 1) ? vk1 : vk0, rd, (e0 + rg * 8) * 2048u + k0 * 2u); }
    }
  };
  const int ph = (rl + cl) & 3;
  TLB(0, 0);
  warm(0); issue(0); issue(1);
  wait_vm<10>(); barrier();
  TLB(0, 1);
  issue(2); issue(3); warm(4); if (ph == 0) warm(8);
  auto iter = [&](int t, auto nw) {
    constexpr int N = decltype(nw)::value;
    int nwarm;
    if (t <= 3) nwarm = 1 + (ph < t || ph == 0 ? 1 : 0);
    else nwarm = (t & 3) != ph ? 1 : 0;
    if (nwarm == 0) wait_vm<N>(); else if (nwarm == 1) wait_vm<N + 1>(); else wait_vm<N + 2>();
    barrier();
    TLB(0, t + 1);
    issue(t + 3);
    if ((t & 3) == ph) warm((t & ~3) + 8);
  };
  for (int t = 1; t < 14; ++t) iter(t, IC<20>{});
  iter(14, IC<18>{});
  for (int t = 15; t < 30; ++t) iter(t, IC<16>{});
  iter(30, IC<8>{}); iter(31, IC<0>{});
  wait_vm<0>();
}
DI void p1_t1(const Args& a, const Ids& id) {
  const int lane = id.lane, wi = id.wid & 1, wj = (id.wid >> 1) & 1, rl = id.idx & 7, cl = id.idx >> 3;
  const int s0 = ((id.xcd & 1) * 8 + rl) * 128 + 64 * wj, head = 2 * ((id.xcd >> 1) * 4 + cl) + wi;
  const unsigned stg = id.lds0 + wj * SLOT40 + 32768 + wi * 4096;
  Cons<32, 3, 2, true, 2> c;
#pragma unroll
  for (int i = 0; i < 3; ++i) c.pa[i] = id.lds0 + ic_raddr<32>(lane, 2 * i + wi);
#pragma unroll
  for (int k = 0; k < 4; ++k) c.qa[k] = id.lds0 + 24576 + kc_raddr<32>(lane, 64 * wj, k);
  c.zero();
  PlainHook hk;
  c.run<16, 4, SLOT40>(hk, 0);
  u16* Qh = a.Q + (size_t)head * 65536 + (size_t)s0 * 32;
  store_rows64<32>(c.acc, 0, QSCALE, stg, lane, (char*)Qh, 64);
  barrier();
  store_rows64<32>(c.acc, 1, 1.0f, stg, lane, (char*)(Qh + 32 * 65536), 64);
  barrier();
  {
    const int d = lane & 31, h = lane >> 5;
#pragma unroll
    for (int jb = 0; jb < 2; ++jb)
#pragma unroll
      for (int g = 0; g < 2; ++g) { const f32x16& v = c.acc[2][jb];
        lds_wr128(stg + d * 128 + (((4 * jb + 2 * g + h) ^ (d & 7)) * 16), u32x4{cvtpk(v[8 * g], v[8 * g + 1]), cvtpk(v[8 * g + 2], v[8 * g + 3]), cvtpk(v[8 * g + 4], v[8 * g + 5]), cvtpk(v[8 * g + 6], v[8 * g + 7])}); }
    u16* Vt = a.Q + (size_t)(64 + head) * 65536 + s0;
#pragma unroll
    for (int m = 0; m < 4; ++m) { const int row = 8 * m + (lane >> 3), cc = lane & 7;
      *(u32x4*)(Vt + (size_t)row * 2048 + cc * 8) = lds_rdu(stg + row * 128 + ((cc ^ (row & 7)) * 16)); }
  }
  for (int t = 18; t < 32; ++t) barrier();
}
DI void p1_t2(const Args& a, const Ids& id) {
  const int lane = id.lane, wi = id.wid & 1, wj = (id.wid >> 1) & 1, rl = id.idx & 7, cl = id.idx >> 3;
  const int s0 = ((id.xcd & 1) * 8 + rl) * 128, eg = ((id.xcd >> 1) * 4 + cl) * 128;
  constexpr int SH = T2_SH, NB = 64 / SH;
  Cons<SH, NB, NB, false> c;
#pragma unroll
  for (int k = 0; k < c.NKS; ++k) { c.pa[k] = id.lds0 + kc_raddr<SH>(lane, 64 * wi, k); c.qa[k] = id.lds0 + 16384 + kc_raddr<SH>(lane, 64 * wj, k); }
  c.zero();
  for (int t = 0; t < 16; ++t) barrier();
  PlainHook hk;
  c.template run<16, 4, SLOT40>(hk, 0);
  const unsigned stg = id.lds0 + wj * SLOT40 + 32768 + wi * 4096;
  const float sc = eg >= 1024 ? -1.0f : 1.0f;
#pragma unroll
  for (int p = 0; p < 2; ++p)
    store_rows128<SH>(c.acc, p, sc, stg, lane, (char*)(a.ABt + (size_t)(eg + 64 * wj + 32 * p) * 2048 + s0 + 64 * wi), 4096);
}

DI void p2_prod(const Args& a, const Ids& id) {
  const int lane = id.lane, w = id.wid - 8, xcd = id.xcd, idx = id.idx;
  const int head = xcd * 4 + (idx >> 3), qb = idx & 7;
  const int half = xcd & 1, tb = idx & 3, eb = idx >> 2, t0 = (xcd >> 1) * 512 + tb * 128, e0 = eb * 128;
  const u16* Kh = a.Q + (size_t)(32 + head) * 65536; const u16* Vt = a.Q + (size_t)(64 + head) * 65536;
  const rsrc_t rk = make_rsrc(Kh, 65536 * 2 + 4096), rv = make_rsrc(Vt, 65536 * 2);
  const rsrc_t rp = make_rsrc(a.ABt + (size_t)half * 1024 * 2048, 1024 * 2048 * 2), rq = make_rsrc(half ? a.Ci : a.Cr, 2048 * 2048 * 2);
  const unsigned vk = k_voff(lane), vv = v_voff(lane), vg0 = kc_voff(lane, 0, 4096), vg1 = kc_voff(lane, 1, 4096);
  const unsigned sp = (unsigned)(e0 + 32 * w) * 4096u, sq = (unsigned)(t0 + 32 * w) * 4096u;
  auto issue = [&](int t) {
    if (t >= 32) return;
    const unsigned base = id.lds0 + (t & 3) * SLOT40, k0 = t * 128;
#pragma unroll
    for (int g = 0; g < 4; ++g) dma16(base + w * 4096 + g * 1024, (g & 1) ? vg1 : vg0, rp, sp + g * 8 * 4096u + k0);
#pragma unroll
    for (int g = 0; g < 4; ++g) dma16(base + 16384 + w * 4096 + g * 1024, (g & 1) ? vg1 : vg0, rq, sq + g * 8 * 4096u + k0);
    dma16(base + 32768 + w * 1024, vk, rk, (unsigned)(64 * t + 32) * 64u + w * 1024u);
    dma16(base + 36864 + w * 1024, vv, rv, w * 8 * 4096u + k0);
  };
  const rsrc_t rpf = w == 0 ? rq : (w < 3 ? rp : make_rsrc(Kh, 33 * 65536 * 2));
  const unsigned pv = w == 0 ? (t0 + 16 * eb + (lane & 15)) * 4096u + (lane >> 4) * 128u
                    : (w < 3 ? (e0 + 32 * tb + (lane & 31)) * 4096u + ((lane >> 5) + 2 * (w - 1)) * 128u : OOB);
  const unsigned pvk = lane < 16 ? 2048u + (qb * 16 + lane) * 128u : OOB;
  const unsigned pvv = lane >= 16 && lane < 32 ? 32u * 65536 * 2 + (qb * 4 + ((lane >> 2) & 3)) * 4096u + (lane & 3) * 128u : OOB;
  auto warm = [&](int b) {
    if (!PF_ON || b >= 8) { l2warm(OOB, rpf, 0); return; }
    if (w < 3) l2warm(pv, rpf, b * 512u); else l2warm(lane < 16 ? pvk + b * 16384u : (pvv == OOB ? OOB : pvv + b * 512u), rpf, 0); };
  const int ph = (tb + eb) & 3;
  TLB(1, 0);
  warm(0); issue(0); issue(1);
  wait_vm<10>(); barrier();
  TLB(1, 1);
  issue(2); issue(3); warm(1); if (ph == 0) warm(2);
  auto iter = [&](int t, auto nw) {
    constexpr int N = decltype(nw)::value;
    int nwarm;
    if (t <= 3) nwarm = 1 + (ph < t || ph == 0 ? 1 : 0); else nwarm = (t & 3) != ph ? 1 : 0;
    if (nwarm == 0) wait_vm<N>(); else if (nwarm == 1) wait_vm<N + 1>(); else wait_vm<N + 2>();
    barrier();
    TLB(1, t + 1);
    issue(t + 3);
    if ((t & 3) == ph) warm((t >> 2) + 2);
  };
  for (int t = 1; t < 30; ++t) iter(t, IC<20>{});
  iter(30, IC<10>{}); iter(31, IC<0>{});
  wait_vm<0>();
  barrier();
  TLB(1, 33);
}
DI void p2_gemm(const Args& a, const Ids& id) {
  const int lane = id.lane, wi = id.wid & 1, wj = (id.wid >> 1) & 1, xcd = id.xcd, idx = id.idx;
  const int half = xcd & 1, t0 = (xcd >> 1) * 512 + (idx & 3) * 128, e0 = (idx >> 2) * 128;
  constexpr int SH = G_SH, NB = 64 / SH;
  Cons<SH, NB, NB, false> c;
#pragma unroll
  for (int k = 0; k < c.NKS; ++k) { c.pa[k] = id.lds0 + kc_raddr<SH>(lane, 64 * wi, k); c.qa[k] = id.lds0 + 16384 + kc_raddr<SH>(lane, 64 * wj, k); }
  c.zero();
  PlainHook hk;
  if (a.mode & 2) { for (int t = 0; t < 32; ++t) barrier(); } else c.template run<32, 4, SLOT40>(hk, 0);
  asm volatile("s_waitcnt lgkmcnt(0)\n\ts_barrier" ::: "memory");
  const unsigned stg = id.lds0 + id.wid * 4096;
#pragma unroll
  for (int p = 0; p < 2; ++p)
    store_rows128<SH>(c.acc, p, 1.0f, stg, lane, (char*)(a.Pp + (size_t)half * 2048 * 1024 + (size_t)(t0 + 64 * wj + 32 * p) * 1024 + e0 + 64 * wi), 2048);
}
DI void p2_attn(const Args& a, const Ids& id) {
  const int lane = id.lane, wid = id.wid, r = lane & 31, h = lane >> 5;
  const int head = id.xcd * 4 + (id.idx >> 3), qb = id.idx & 7;
  const u16* Qh = a.Q + (size_t)head * 65536; const u16* Kh = Qh + 32 * 65536; const u16* Vt = Kh + 32 * 65536;
  AttnWave w;
  const int q0 = qb * 256 + wid * 64;
#pragma unroll
  for (int b = 0; b < 2; ++b)
#pragma unroll
    for (int s = 0; s < 2; ++s) w.q[b][s] = *(const bf16x8*)(Qh + (size_t)(q0 + 32 * b + r) * 32 + 16 * s + 8 * h);
  w.kf[0] = *(const bf16x8*)(Kh + (size_t)r * 32 + 8 * h); w.kf[1] = *(const bf16x8*)(Kh + (size_t)r * 32 + 16 + 8 * h);
#pragma unroll
  for (int b = 0; b < 2; ++b) {
    f32x16 s = mfma32(w.kf[0], w.q[b][0], f32x16{}); s = mfma32(w.kf[1], w.q[b][1], s);
    const float nm = -(rowmax16(s) + MARGIN);
#pragma unroll
    for (int i = 0; i < 16; ++i) w.NM[b][i] = nm;
    if (b == 0) w.S[0] = s + nm;
    w.O[b] = f32x16{}; w.L[b] = f32x4{};
  }
  w.S[1] = f32x16{};
  w.P[1][0] = u32x4{}; w.P[1][1] = u32x4{}; w.P[0][0] = u32x4{}; w.P[0][1] = u32x4{};
  w.vf[0] = bf16x8{}; w.vf[1] = bf16x8{};
  { const int m = lane & 15; const __bf16 one = (__bf16)1.0f, z = (__bf16)0.0f; const bool lo = (m == 0 || m == 4), hi = (m == 8 || m == 12);
    w.asel = bf16x8{lo ? one : z, lo ? one : z, lo ? one : z, lo ? one : z, hi ? one : z, hi ? one : z, hi ? one : z, hi ? one : z};
    w.sidx = (m == 4 || m == 12) ? 0xEEEE : 0x4444; }
  w.ka0 = id.lds0 + 32768 + k_raddr(lane, 0); w.ka1 = id.lds0 + 32768 + k_raddr(lane, 1);
  w.va0 = id.lds0 + 36864 + v_raddr(lane, 0); w.va1 = id.lds0 + 36864 + v_raddr(lane, 1);
  if (a.mode & 1) __builtin_amdgcn_s_setprio(1);
  if (a.mode & 4) { for (int t = 0; t < 32; ++t) barrier(); } else w.run<4, SLOT40>();
  asm volatile("s_waitcnt lgkmcnt(0)\n\ts_barrier" ::: "memory");
  const unsigned stg = id.lds0 + wid * 4096;
  const int ln = fresh_lane(), r2 = ln & 31, h2 = ln >> 5;
#pragma unroll
  for (int b = 0; b < 2; ++b) {
    const int src = (r2 & 15) + (r2 >> 4) * 32;
    float l = __shfl(w.L[b][0], src) + __shfl(w.L[b][0], src + 16);
    float amax = 0.f;
#pragma unroll
    for (int i = 0; i < 16; ++i) amax = __builtin_fmaxf(amax, __builtin_fabsf(w.O[b][i]));
    bool nan = false;
#pragma unroll
    for (int i = 0; i < 16; ++i) nan |= !(w.O[b][i] == w.O[b][i]);
    const bool bad = !(a.mode & 4) && (!(l < 0x1p126f) || !(amax < 0x1p126f) || nan || (amax < 0x1p-100f) || a.force_slow);
    if (__any(bad)) attn_slow(Kh, Vt, w.q[b], ln, w.O[b], l);
    const float inv = 1.0f / l;
    const f32x16 v = w.O[b] * inv;
    lds_wr128(stg + b * 2048 + r2 * 64 + ((h2 ^ ((r2 >> 1) & 3)) * 16), u32x4{cvtpk(v[0], v[1]), cvtpk(v[2], v[3]), cvtpk(v[4], v[5]), cvtpk(v[6], v[7])});
    lds_wr128(stg + b * 2048 + r2 * 64 + (((2 + h2) ^ ((r2 >> 1) & 3)) * 16), u32x4{cvtpk(v[8], v[9]), cvtpk(v[10], v[11]), cvtpk(v[12], v[13]), cvtpk(v[14], v[15])});
  }
#pragma unroll
  for (int m = 0; m < 4; ++m) { const int row = 16 * m + (ln >> 2), c = ln & 3;
    *(u32x4*)(a.O + (size_t)(q0 + row) * 1024 + head * 32 + c * 8) = lds_rdu(stg + row * 64 + ((c ^ ((row >> 1) & 3)) * 16)); }
}

constexpr int P3_NS = 6, P3_SLOT = 24576, P3_STG = P3_NS * P3_SLOT;
DI unsigned addbf2(unsigned x, unsigned y) { return cvtpk(__uint_as_float(x << 16) + __uint_as_float(y << 16), __uint_as_float(x & 0xffff0000u) + __uint_as_float(y & 0xffff0000u)); }
DI void p3_prod(const Args& a, const Ids& id) {
  const int lane = id.lane, w = id.wid - 8, nl = id.idx & 3, sl = id.idx >> 2;
  const int n0 = ((id.xcd & 1) * 4 + nl) * 128, s0 = ((id.xcd >> 1) * 8 + sl) * 64;
  const rsrc_t ro = make_rsrc(a.O, 2048 * 1024 * 2), rw = make_rsrc(a.wo, 1024 * 1024 * 2);
  const unsigned vk0 = kc_voff(lane, 0, 2048), vk1 = kc_voff(lane, 1, 2048), vi0 = ic_voff(lane, 0, 2048), vi1 = ic_voff(lane, 1, 2048);
  auto issue = [&](int t) {
    const unsigned base = id.lds0 + (t % P3_NS) * P3_SLOT, k0 = t * 64;
#pragma unroll
    for (int p = 0; p < 2; ++p)
#pragma unroll
      for (int g = 0; g < 2; ++g) { const unsigned kg = 2 * w + g; dma16(base + p * 8192 + kg * 1024, g ? vi1 : vi0, rw, (k0 + kg * 8) * 2048u + (n0 + p * 64) * 2u); }
#pragma unroll
    for (int g = 0; g < 2; ++g) { const unsigned rg = 2 * w + g; dma16(base + 16384 + rg * 1024, g ? vk1 : vk0, ro, (s0 + rg * 8) * 2048u + k0 * 2u); }
  };
  if (PF_ON) {
    l2warm((128 * sl + (w & 1) * 64 + lane) * 2048u + n0 * 2 + (w >> 1) * 128u, rw, 0);
    l2warm((s0 + 16 * nl + (lane & 15)) * 2048u + ((lane >> 4) + 4 * w) * 128u, ro, 0);
  }
  TLB(2, 0);
  issue(0); issue(1);
  wait_vm<6>(); barrier();
  TLB(2, 1);
  issue(2); issue(3); issue(4); issue(5);
  for (int t = 1; t < 16; ++t) {
    if (t <= 11) wait_vm<24>(); else if (t == 12) wait_vm<18>(); else if (t == 13) wait_vm<12>(); else if (t == 14) wait_vm<6>(); else wait_vm<0>();
    barrier();
    TLB(2, t + 1);
    if (t + 5 < 16) issue(t + 5);
  }
}
DI void p3_comb(const Args& a, const Ids& id) {
  const size_t e = (size_t)blockIdx.x * 8192 + (id.wid * 64 + id.lane) * 8;
#pragma unroll
  for (int i = 0; i < 4; ++i) {
    const size_t o = e + i * 2048;
    const u32x4 x = *(const u32x4*)(a.Pp + o), y = *(const u32x4*)(a.Pp + 2048 * 1024 + o);
    const u32x4 z = {addbf2(x[0], y[0]), addbf2(x[1], y[1]), addbf2(x[2], y[2]), addbf2(x[3], y[3])};
    *(u32x4*)(a.out + (o >> 10) * 2048 + 1024 + (o & 1023)) = z;
  }
}
DI void p3_gemm(const Args& a, const Ids& id) {
  const int lane = id.lane, wi = id.wid & 1, wj = (id.wid >> 1) & 1, nl = id.idx & 3, sl = id.idx >> 2;
  const int n0 = ((id.xcd & 1) * 4 + nl) * 128, s0 = ((id.xcd >> 1) * 8 + sl) * 64;
  constexpr int SH = T5_SH, NI = 64 / SH, NJ = 32 / SH;
  Cons<SH, NI, NJ, true> c;
#pragma unroll
  for (int i = 0; i < NI; ++i) c.pa[i] = id.lds0 + ic_raddr<SH>(lane, NI * wi + i);
#pragma unroll
  for (int k = 0; k < c.NKS; ++k) c.qa[k] = id.lds0 + 16384 + kc_raddr<SH>(lane, 32 * wj, k);
  c.zero();
  PlainHook hk;
  c.template run<16, P3_NS, P3_SLOT>(hk, 0);
  store_rows128<SH>(c.acc, 0, 1.0f, id.lds0 + P3_STG + (id.wid & 3) * 4096, lane, (char*)(a.out + (size_t)(s0 + 32 * wj) * 2048 + n0 + 64 * wi), 4096);
}

DI Ids make_ids(char* lds) { Ids id; id.lane = threadIdx.x & 63; id.wid = __builtin_amdgcn_readfirstlane(threadIdx.x >> 6); id.xcd = blockIdx.x & 7; id.idx = blockIdx.x >> 3; id.lds0 = (unsigned)(size_t)(LAS char*)lds; return id; }
#define KSTAMP0()
#define KSTAMP1(W, off)
__global__ __launch_bounds__(768, 3) void k1_kernel(Args a) {
  extern __shared__ __attribute__((aligned(16))) char lds[];
  const Ids id = make_ids(lds); KSTAMP0();
  if (id.wid >= 8) p1_prod(a, id); else if (id.wid >= 4) { p1_t2(a, id); KSTAMP1(4, 0); } else p1_t1(a, id);
}
__global__ __launch_bounds__(768, 3) void k2_kernel(Args a) {
  extern __shared__ __attribute__((aligned(16))) char lds[];
  const Ids id = make_ids(lds); KSTAMP0();
  if (id.wid >= 8) p2_prod(a, id); else if (id.wid >= 4) { p2_gemm(a, id); } else { p2_attn(a, id); KSTAMP1(0, 1024); }
}
__global__ __launch_bounds__(768, 3) void k3_kernel(Args a) {
  extern __shared__ __attribute__((aligned(16))) char lds[];
  const Ids id = make_ids(lds); KSTAMP0();
  if (id.wid >= 8) p3_prod(a, id); else if (id.wid >= 4) { p3_gemm(a, id); KSTAMP1(4, 2048); } else p3_comb(a, id);
}
constexpr int LDS_ALL = 163840;

extern "C" void kernel_launch(void* const* d_in, const int* in_sizes, int n_in, void* d_out, int out_size, void* d_ws, size_t ws_size, hipStream_t stream) {
  u16* ws = (u16*)d_ws; const size_t M = (size_t)2048 * 1024;
  Args a;
  a.Ax = (const u16*)d_in[0]; a.wqkv = (const u16*)d_in[1]; a.wo = (const u16*)d_in[2]; a.Bx = (const u16*)d_in[3];
  a.Cr = (const u16*)d_in[4]; a.Ci = (const u16*)d_in[5]; a.Wr = (const u16*)d_in[6]; a.Wi = (const u16*)d_in[7];
  a.Q = ws; a.ABt = ws + 3 * M; a.O = ws + 5 * M; a.Pp = ws + 6 * M; a.out = (u16*)d_out; a.dbg = nullptr; a.force_slow = 0; a.mode = 0;
  (void)hipFuncSetAttribute((const void*)k1_kernel, hipFuncAttributeMaxDynamicSharedMemorySize, LDS_ALL);
  (void)hipFuncSetAttribute((const void*)k2_kernel, hipFuncAttributeMaxDynamicSharedMemorySize, LDS_ALL);
  (void)hipFuncSetAttribute((const void*)k3_kernel, hipFuncAttributeMaxDynamicSharedMemorySize, LDS_ALL);
  k1_kernel<<<256, 768, LDS_ALL, stream>>>(a);
  k2_kernel<<<256, 768, LDS_ALL, stream>>>(a);
  k3_kernel<<<256, 768, LDS_ALL, stream>>>(a);
}
```

```cpp
#include <hip/hip_runtime.h>
typedef unsigned short u16;
typedef __bf16 bf16x8 __attribute__((ext_vector_type(8)));
typedef __bf16 bf16x16 __attribute__((ext_vector_type(16)));
typedef __bf16 bf16x2 __attribute__((ext_vector_type(2)));
typedef short s16x4 __attribute__((ext_vector_type(4)));
typedef short s16x8 __attribute__((ext_vector_type(8)));
typedef float f32x2 __attribute__((ext_vector_type(2)));
typedef float f32x4 __attribute__((ext_vector_type(4)));
typedef float f32x16 __attribute__((ext_vector_type(16)));
typedef unsigned u32x4 __attribute__((ext_vector_type(4)));
typedef unsigned u32x2 __attribute__((ext_vector_type(2)));
#define LAS __attribute__((address_space(3)))
#define DI __device__ __forceinline__
#define SBAR() __builtin_amdgcn_sched_barrier(0)
#define PIN(x) asm volatile("" : "+v"(x))
typedef __amdgpu_buffer_rsrc_t rsrc_t;

DI int fresh_lane() { int l; asm volatile("v_mbcnt_lo_u32_b32 %0, -1, 0\n\tv_mbcnt_hi_u32_b32 %0, -1, %0" : "=v"(l)); return l; }
DI rsrc_t make_rsrc(const void* p, unsigned bytes) { return __builtin_amdgcn_make_buffer_rsrc((void*)p, 0, bytes, 0x00020000); }
DI void dma16(unsigned ldsaddr, unsigned voff, rsrc_t rs, unsigned soff) {
  asm volatile("s_mov_b32 m0, %0\n\ts_nop 0\n\tbuffer_load_dwordx4 %1, %2, %3 offen lds" ::"s"(ldsaddr), "v"(voff), "s"(rs), "s"(soff) : "memory");
}
DI void dma16s(unsigned ldsaddr, unsigned voff, rsrc_t rs, unsigned soff) {
  asm volatile("s_mov_b32 m0, %0\n\ts_nop 0\n\tbuffer_load_dwordx4 %1, %2, %3 offen sc1 lds" ::"s"(ldsaddr), "v"(voff), "s"(rs), "s"(soff) : "memory");
}
DI u32x4 ld16s(rsrc_t rs, unsigned voff, unsigned soff) { return __builtin_amdgcn_raw_buffer_load_b128(rs, voff, soff, 16); }
DI void st16s(rsrc_t rs, unsigned voff, unsigned soff, u32x4 v) { __builtin_amdgcn_raw_buffer_store_b128(v, rs, voff, soff, 16); }
DI void l2warm(unsigned voff, rsrc_t rs, unsigned soff) {
  asm volatile("buffer_load_dword v167, %0, %1, %2 offen sc1" ::"v"(voff), "s"(rs), "s"(soff) : "memory", "v167");
}
constexpr unsigned OOB = 0x7ff00000u;
template <int N> DI void wait_vm() { asm volatile("s_waitcnt vmcnt(%0)" ::"n"(N) : "memory"); }
DI void wait_lgkm0() { asm volatile("s_waitcnt lgkmcnt(0)" ::: "memory"); }
DI void barrier() { asm volatile("s_barrier" ::: "memory"); }
DI unsigned cvtpk(float lo, float hi) { f32x2 v = {lo, hi}; bf16x2 b = __builtin_convertvector(v, bf16x2); return __builtin_bit_cast(unsigned, b); }
DI f32x16 mfma32(bf16x8 a, bf16x8 b, f32x16 c) { return __builtin_amdgcn_mfma_f32_32x32x16_bf16(a, b, c, 0, 0, 0); }
DI f32x4 mfma16(bf16x8 a, bf16x8 b, f32x4 c) { return __builtin_amdgcn_mfma_f32_16x16x32_bf16(a, b, c, 0, 0, 0); }
DI bf16x8 lds_rd128(unsigned addr) { return *(const LAS bf16x8*)(size_t)addr; }
DI u32x4 lds_rdu(unsigned addr) { return *(const LAS u32x4*)(size_t)addr; }
DI void lds_wr64(unsigned addr, u32x2 v) { *(LAS u32x2*)(size_t)addr = v; }
DI void lds_wr128(unsigned addr, u32x4 v) { *(LAS u32x4*)(size_t)addr = v; }
DI s16x4 lds_rdtr(unsigned addr) { return __builtin_amdgcn_ds_read_tr16_b64_v4i16((LAS s16x4*)(size_t)addr); }
DI bf16x8 cat8(s16x4 a, s16x4 b) { s16x8 r = __builtin_shufflevector(a, b, 0, 1, 2, 3, 4, 5, 6, 7); return __builtin_bit_cast(bf16x8, r); }

DI unsigned kc_voff(int lane, int par, unsigned ldb) { return (unsigned)(lane >> 3) * ldb + (unsigned)(((lane & 7) ^ (((lane >> 4) + 4 * par) & 7)) * 16); }
DI unsigned ic_voff(int lane, int par, unsigned ldb) { const int c = lane & 7, u = (c >> 1) ^ (((lane >> 4) & 1) << 1) ^ par; return (unsigned)(lane >> 3) * ldb + (unsigned)((u * 2 + (c & 1)) * 16); }
template <int SH> DI unsigned kc_raddr(int lane, int row0, int ks) {
  const int row = row0 + (lane & (SH - 1)), ch = SH == 16 ? 4 * ks + (lane >> 4) : 2 * ks + (lane >> 5);
  return (unsigned)(row * 128 + ((ch ^ ((row >> 1) & 7)) * 16)); }
template <int SH> DI unsigned ic_raddr(int lane, int cb) {
  const int g = lane >> 4, q = (lane & 15) >> 2, p = lane & 3;
  const int kg = SH == 16 ? g : (g >> 1), c16 = SH == 16 ? cb : 2 * cb + (g & 1);
  const int m = (((q >> 1) & 1) << 1) ^ (kg & 1);
  return (unsigned)((c16 >> 2) * 8192 + (8 * kg + q) * 128 + (((c16 & 3) ^ m) * 32) + p * 8);
}
template <int SH> struct AccT; template <> struct AccT<16> { typedef f32x4 t; }; template <> struct AccT<32> { typedef f32x16 t; };

template <int SH, int NI, int NJ, bool PIC, int SWAPI = -1> struct Cons {
  static constexpr int NKS = SH == 16 ? 2 : 4, NPA = PIC ? NI : NKS, NM = NI * NJ, BLK = SH * 128, KSO = SH == 16 ? 4096 : 2048;
  typedef typename AccT<SH>::t acc_t;
  unsigned pa[NPA], qa[NKS];
  acc_t acc[NI][NJ];
  bf16x8 p0[NI], q0[NJ], p1[NI], q1[NJ];
  static constexpr int PI = PIC ? 2 : 1, NIT = NJ + NI * PI;
  s16x4 tlo;
  template <int KS, int K> DI void rd(bf16x8 (&p)[NI], bf16x8 (&q)[NJ]) {
    if constexpr (K == 0) q[0] = lds_rd128(qa[KS]);
    else if constexpr (K > PI && K < PI + NJ) q[K - PI] = lds_rd128(qa[KS] + (K - PI) * BLK);
    else { constexpr int r = K <= PI ? K - 1 : K - NJ, i = r / PI, hf = r % PI;
      if constexpr (!PIC) p[i] = lds_rd128(pa[KS] + i * BLK);
      else if constexpr (hf == 0) tlo = lds_rdtr(pa[i] + KS * KSO);
      else p[i] = cat8(tlo, lds_rdtr(pa[i] + KS * KSO + 512)); }
  }
  template <int I, int J> DI void mma(bf16x8 a, bf16x8 b) {
    if constexpr (SH == 16) acc[I][J] = mfma16(a, b, acc[I][J]);
    else if constexpr (I == SWAPI) acc[I][J] = mfma32(b, a, acc[I][J]);
    else acc[I][J] = mfma32(a, b, acc[I][J]); }
  template <int KS, bool RD, int G0, bool FIRST, int M, class H> DI void grp(bf16x8 (&cp)[NI], bf16x8 (&cq)[NJ], bf16x8 (&np)[NI], bf16x8 (&nq)[NJ], H& hk, int delta) {
    if constexpr (M < NM) {
      mma<M / NJ, M % NJ>(cp[M / NJ], cq[M % NJ]); SBAR();
      if constexpr (FIRST && M == 0) { advance(delta); hk.sync(); SBAR(); }
      constexpr int RG = NM - 1 < NIT ? (NM > 1 ? NM - 1 : 1) : NIT;
      constexpr int lo = (M * NIT + RG - 1) / RG, hi = M + 1 < RG ? ((M + 1) * NIT + RG - 1) / RG : NIT;
      if constexpr (RD && M < RG) {
        if constexpr (lo < hi) rd<KS, lo>(np, nq);
        if constexpr (lo + 1 < hi) rd<KS, lo + 1>(np, nq);
        if constexpr (lo + 2 < hi) rd<KS, lo + 2>(np, nq);
        if constexpr (lo + 3 < hi) rd<KS, lo + 3>(np, nq);
        if constexpr (lo + 4 < hi) rd<KS, lo + 4>(np, nq);
      }
      hk.template at<G0 + M>();
      SBAR();
      grp<KS, RD, G0, FIRST, M + 1>(cp, cq, np, nq, hk, delta);
    }
  }
  DI void advance(int delta) {
#pragma unroll
    for (int i = 0; i < NPA; ++i) { pa[i] += delta; PIN(pa[i]); }
#pragma unroll
    for (int i = 0; i < NKS; ++i) { qa[i] += delta; PIN(qa[i]); }
  }
  template <int G, class H> DI void group(H& hk, int delta = 0) {
    if constexpr ((G & 1) == 0) grp<G, true, G * NM, G == 0, 0>(p1, q1, p0, q0, hk, delta);
    else grp<G, true, G * NM, false, 0>(p0, q0, p1, q1, hk, 0);
  }
  template <class H> DI void tail(H& hk) { grp<0, false, 1000, false, 0>(p1, q1, p0, q0, hk, 0); }
  template <int NT, int NS, int SLOT, class H> DI void run(H& hk, int s0) {
    int slot = s0;
    group<0>(hk, 0);
    if constexpr (NKS == 4) group<1>(hk);
    for (int t = 1; t < NT; ++t) {
      const int d = slot == NS - 1 ? -(NS - 1) * SLOT : SLOT; slot = slot == NS - 1 ? 0 : slot + 1;
      if constexpr (NKS == 4) { group<2>(hk); group<3>(hk); group<0>(hk, d); group<1>(hk); }
      else { group<1>(hk); group<0>(hk, d); }
    }
    if constexpr (NKS == 4) { group<2>(hk); group<3>(hk); } else group<1>(hk);
    tail(hk);
  }
  DI void zero() {
#pragma unroll
    for (int i = 0; i < NI; ++i)
#pragma unroll
      for (int j = 0; j < NJ; ++j) acc[i][j] = acc_t{};
#pragma unroll
    for (int i = 0; i < NI; ++i) p1[i] = bf16x8{};
#pragma unroll
    for (int j = 0; j < NJ; ++j) q1[j] = bf16x8{};
  }
};
struct PlainHook { template <int M> DI void at() {} DI void sync() { asm volatile("s_waitcnt lgkmcnt(0)\n\ts_barrier" ::: "memory"); } };
struct NoSyncHook { template <int M> DI void at() {} DI void sync() { asm volatile("s_waitcnt lgkmcnt(0)" ::: "memory"); } };

DI u32x2 pk4(float a, float b, float c, float d, float sc) { return u32x2{cvtpk(a * sc, b * sc), cvtpk(c * sc, d * sc)}; }
template <bool SC1> DI void st16(rsrc_t rs, unsigned voff, unsigned soff, u32x4 v) { __builtin_amdgcn_raw_buffer_store_b128(v, rs, voff, soff, SC1 ? 16 : 0); }
template <bool SC1> DI void flush64(unsigned stg, int lane, rsrc_t rs, unsigned soff, unsigned ldb) {
#pragma unroll
  for (int m = 0; m < 4; ++m) { const int row = 16 * m + (lane >> 2), c = lane & 3;
    st16<SC1>(rs, row * ldb + c * 16, soff, lds_rdu(stg + row * 64 + ((c ^ ((row >> 1) & 3)) * 16))); }
}
template <bool SC1> DI void flush128(unsigned stg, int lane, rsrc_t rs, unsigned soff, unsigned ldb) {
#pragma unroll
  for (int m = 0; m < 4; ++m) { const int row = 8 * m + (lane >> 3), c = lane & 7;
    st16<SC1>(rs, row * ldb + c * 16, soff, lds_rdu(stg + row * 128 + ((c ^ (row & 7)) * 16))); }
}
template <bool SC1, int NI, int NJ> DI void store_rows64(const f32x16 (&acc)[NI][NJ], int i32, float sc, unsigned stg, int lane, rsrc_t rs, unsigned soff, unsigned ldb) {
  const int j = lane & 31, h = lane >> 5;
#pragma unroll
  for (int jj = 0; jj < 2; ++jj)
#pragma unroll
    for (int t = 0; t < 4; ++t) { const f32x16& v = acc[i32][jj]; const int row = 32 * jj + j;
      lds_wr64(stg + row * 64 + ((t ^ ((row >> 1) & 3)) * 16) + h * 8, pk4(v[4 * t], v[4 * t + 1], v[4 * t + 2], v[4 * t + 3], sc)); }
  flush64<SC1>(stg, lane, rs, soff, ldb);
}
template <bool SC1, int SH, int NI, int NJ, class A> DI void store_rows128(const A (&acc)[NI][NJ], int j32, float sc, unsigned stg, int lane, rsrc_t rs, unsigned soff, unsigned ldb) {
  if constexpr (SH == 16) {
    const int j = lane & 15, g = lane >> 4;
#pragma unroll
    for (int jj = 0; jj < 2; ++jj)
#pragma unroll
      for (int ii = 0; ii < 4; ++ii) { const A& v = acc[ii][2 * j32 + jj]; const int row = 16 * jj + j;
        lds_wr64(stg + row * 128 + (((2 * ii + (g >> 1)) ^ (row & 7)) * 16) + (g & 1) * 8, pk4(v[0], v[1], v[2], v[3], sc)); }
  } else {
    const int row = lane & 31, h = lane >> 5;
#pragma unroll
    for (int ii = 0; ii < 2; ++ii)
#pragma unroll
      for (int t = 0; t < 4; ++t) { const A& v = acc[ii][j32];
        lds_wr64(stg + row * 128 + (((4 * ii + t) ^ (row & 7)) * 16) + h * 8, pk4(v[4 * t], v[4 * t + 1], v[4 * t + 2], v[4 * t + 3], sc)); }
  }
  flush128<SC1>(stg, lane, rs, soff, ldb);
}
#define STAMP()
#define STAMP_END(W)

DI float ex2(float x) { return __builtin_amdgcn_exp2f(x); }
DI bf16x16 cat16(u32x4 a, u32x4 b) { typedef unsigned u32x8 __attribute__((ext_vector_type(8))); u32x8 r = __builtin_shufflevector(a, b, 0, 1, 2, 3, 4, 5, 6, 7); return __builtin_bit_cast(bf16x16, r); }
DI int iperm(int r) { const int b = r & 3, h = (r >> 2) & 1, t = r >> 3; return b + 4 * (t & 1) + 8 * h + 16 * (t >> 1); }
DI unsigned k_voff(int lane) { return (unsigned)((lane >> 2) * 64 + (((lane & 3) ^ ((lane >> 4) & 3)) * 16)); }
DI unsigned v_voff(int lane) { const int c = lane & 7; return (unsigned)((lane >> 3) * 4096 + ((c & 4) | ((c & 3) ^ ((lane >> 4) & 3))) * 16); }
DI unsigned k_raddr(int lane, int s) { const int r = lane & 31, h = lane >> 5; return (unsigned)(r * 64 + (((2 * s + h) ^ ((r >> 2) & 3)) * 16)); }
DI unsigned v_raddr(int lane, int s) { const int d = iperm(lane & 31), h = lane >> 5; return (unsigned)(d * 128 + (((2 * s + h) ^ ((d >> 1) & 3)) * 16)); }
DI float swap32_max(float m) { auto rr = __builtin_amdgcn_permlane32_swap(__float_as_uint(m), __float_as_uint(m), false, false); return __builtin_fmaxf(__uint_as_float(rr[0]), __uint_as_float(rr[1])); }
DI float swap32_sum(float m) { auto rr = __builtin_amdgcn_permlane32_swap(__float_as_uint(m), __float_as_uint(m), false, false); return __uint_as_float(rr[0]) + __uint_as_float(rr[1]); }
DI float rowmax16(const f32x16& s) { float m = s[0];
#pragma unroll
  for (int i = 1; i < 16; ++i) m = __builtin_fmaxf(m, s[i]);
  return swap32_max(m); }
constexpr float MARGIN = 64.0f;

struct AttnWave {
  f32x16 S[2], NM[2], O[2]; f32x4 L[2];
  u32x4 P[2][2];
  bf16x8 q[2][2], kf[2], vf[2];
  bf16x8 asel; int sidx;
  unsigned ka0, ka1, va0, va1;
  template <int X> DI void exp4(int i) { S[X][i] = ex2(S[X][i]); S[X][i + 1] = ex2(S[X][i + 1]); S[X][i + 2] = ex2(S[X][i + 2]); S[X][i + 3] = ex2(S[X][i + 3]); }
  template <int X> DI void cvt2(int i) { P[X][i >> 3][(i >> 1) & 3] = cvtpk(S[X][i], S[X][i + 1]); P[X][i >> 3][((i >> 1) & 3) + 1] = cvtpk(S[X][i + 2], S[X][i + 3]); }
  template <int X, int RD> DI void slot(int delta = 0) {
    constexpr int Y = X ^ 1, B = RD - 1;
    S[Y] = mfma32(kf[0], q[Y][0], NM[Y]); SBAR();
    if constexpr (RD == 1) { ka0 += delta; ka1 += delta; va0 += delta; va1 += delta; PIN(ka0); PIN(ka1); PIN(va0); PIN(va1); asm volatile("s_waitcnt lgkmcnt(0)\n\ts_barrier" ::: "memory"); SBAR(); }
    if constexpr (RD) { kf[0] = lds_rd128(ka0 + B * 2048); SBAR(); }
    exp4<X>(0); PIN(S[X]); SBAR();
    S[Y] = mfma32(kf[1], q[Y][1], S[Y]); SBAR();
    if constexpr (RD) { kf[1] = lds_rd128(ka1 + B * 2048); SBAR(); }
    exp4<X>(4); cvt2<X>(0); PIN(S[X]); PIN(P[X][0]); SBAR();
    O[Y] = mfma32(vf[0], __builtin_bit_cast(bf16x8, P[Y][0]), O[Y]); SBAR();
    if constexpr (RD) { vf[0] = lds_rd128(va0 + B * 64); SBAR(); }
    exp4<X>(8); cvt2<X>(4); PIN(S[X]); PIN(P[X][0]); SBAR();
    O[Y] = mfma32(vf[1], __builtin_bit_cast(bf16x8, P[Y][1]), O[Y]); SBAR();
    if constexpr (RD) { vf[1] = lds_rd128(va1 + B * 64); SBAR(); }
    exp4<X>(12); cvt2<X>(8); PIN(S[X]); PIN(P[X][1]); SBAR();
    L[Y] = __builtin_amdgcn_smfmac_f32_16x16x64_bf16(asel, cat16(P[Y][0], P[Y][1]), L[Y], sidx, 0, 0); SBAR();
    cvt2<X>(12); PIN(P[X][1]); SBAR();
  }
  template <int NS, int SLOT> DI void run(int sl) {
    slot<0, 1>(0); slot<1, 0>();
    for (int t = 1; t < 32; ++t) {
      const int d = sl == NS - 1 ? -(NS - 1) * SLOT : SLOT; sl = sl == NS - 1 ? 0 : sl + 1;
      slot<0, 2>(); slot<1, 0>(); slot<0, 1>(d); slot<1, 0>();
    }
    slot<0, 2>(); slot<1, 0>();
    O[1] = mfma32(vf[0], __builtin_bit_cast(bf16x8, P[1][0]), O[1]);
    O[1] = mfma32(vf[1], __builtin_bit_cast(bf16x8, P[1][1]), O[1]);
    L[1] = __builtin_amdgcn_smfmac_f32_16x16x64_bf16(asel, cat16(P[1][0], P[1][1]), L[1], sidx, 0, 0);
  }
};

DI void attn_slow(rsrc_t rk, rsrc_t rv, const bf16x8 (&q)[2], int lane, f32x16& O, float& l) {
  const int r = lane & 31, h = lane >> 5;
  float m = -INFINITY; l = 0.f; O = f32x16{};
  const unsigned ko = r * 64 + h * 16, vo = iperm(r) * 4096 + h * 16;
  for (int j = 0; j < 64; ++j) {
    f32x16 s = mfma32(__builtin_bit_cast(bf16x8, ld16s(rk, ko, j * 2048)), q[0], f32x16{});
    s = mfma32(__builtin_bit_cast(bf16x8, ld16s(rk, ko + 32, j * 2048)), q[1], s);
    const float mn = __builtin_fmaxf(m, rowmax16(s));
    const float f = ex2(m - mn); m = mn;
    float ps = 0.f; u32x4 p0, p1;
#pragma unroll
    for (int i = 0; i < 16; ++i) s[i] = ex2(s[i] - mn);
#pragma unroll
    for (int i = 0; i < 4; ++i) { p0[i] = cvtpk(s[2 * i], s[2 * i + 1]); p1[i] = cvtpk(s[8 + 2 * i], s[9 + 2 * i]); }
#pragma unroll
    for (int i = 0; i < 4; ++i) { ps += __uint_as_float(p0[i] << 16) + __uint_as_float(p0[i] & 0xffff0000u) + __uint_as_float(p1[i] << 16) + __uint_as_float(p1[i] & 0xffff0000u); }
    l = l * f + swap32_sum(ps);
    O = O * f;
    O = mfma32(__builtin_bit_cast(bf16x8, ld16s(rv, vo, j * 64)), __builtin_bit_cast(bf16x8, p0), O);
    O = mfma32(__builtin_bit_cast(bf16x8, ld16s(rv, vo + 32, j * 64)), __builtin_bit_cast(bf16x8, p1), O);
  }
}

constexpr float QSCALE = 0.17677669529663687f * 1.4426950408889634f;
constexpr int SLOT = 40960, LDS_ALL = 163840;
#define S_A 26
#define S_T4 39
constexpr int S_T2 = 16, S_T5 = S_T4 + 32, S_END = S_T5 + 16;
static_assert(S_A >= 25 && S_A <= 31 && S_T4 >= 38 && S_A + 32 + 6 <= S_T5 - 3 && S_T4 + 24 <= S_T5 - 8 && S_A + 29 <= S_T5 - 8 && S_T4 - 8 >= S_A - 2, "schedule");
#define T2_SH 32
#define G_SH 16
#define PF_ON 1
struct Args {
  const u16 *Ax, *wqkv, *wo, *Bx, *Cr, *Ci, *Wr, *Wi;
  u16 *Q, *ABt, *O; float* X; u16* out;
  unsigned* cnt;
  unsigned long long* dbg; int force_slow; int mode;
};
constexpr int C_QKV = 0, C_AB = 32, C_O = 64, C_P = 96, C_TMO = 96 + 512, C_LINES = C_TMO + 1;
struct Ids { int lane, wid, xcd, idx; unsigned lds0; };
#define TLB(t)
#define TLW(w, t)
DI void idle(int n) { for (int i = 0; i < n; ++i) barrier(); }
DI void signal(unsigned* cnt, int line, int lane) { wait_vm<0>(); if (lane == 0) __hip_atomic_fetch_add(cnt + line * 32, 1u, __ATOMIC_RELAXED, __HIP_MEMORY_SCOPE_AGENT); }
DI void await(unsigned* cnt, int line0, int n, unsigned target, int lane) {
  unsigned* p = cnt + (line0 + (lane < n ? lane : 0)) * 32;
  const unsigned long long t0 = __builtin_amdgcn_s_memrealtime();
  for (;;) {
    const unsigned v = __hip_atomic_load(p, __ATOMIC_RELAXED, __HIP_MEMORY_SCOPE_AGENT);
    if (__all(v >= target)) break;
    if (__builtin_amdgcn_s_memrealtime() - t0 > 2000000ull) { if (lane == 0) __hip_atomic_store(cnt + C_TMO * 32, 1u, __ATOMIC_RELAXED, __HIP_MEMORY_SCOPE_AGENT); break; }
    __builtin_amdgcn_s_sleep(1);
  }
}
constexpr __host__ __device__ bool in_(int s, int a, int n) { return s >= a && s < a + n; }
constexpr __host__ __device__ int npieces(int s) { return (s < 16 ? 10 : 0) + ((in_(s, S_T2, 16) || in_(s, S_T4, 32)) ? 8 : 0) + (in_(s, S_A, 32) ? 2 : 0) + (in_(s, S_T5, 16) ? 6 : 0); }

template <int IMM, bool SC1> DI void piece(unsigned sb, unsigned voff, rsrc_t rs, unsigned soff) {
  if constexpr (SC1) asm volatile("s_add_u32 m0, %0, %1\n\ts_nop 0\n\tbuffer_load_dwordx4 %2, %3, %4 offen sc1 lds" ::"s"(sb), "n"(IMM), "v"(voff), "s"(rs), "s"(soff) : "memory", "scc");
  else asm volatile("s_add_u32 m0, %0, %1\n\ts_nop 0\n\tbuffer_load_dwordx4 %2, %3, %4 offen lds" ::"s"(sb), "n"(IMM), "v"(voff), "s"(rs), "s"(soff) : "memory", "scc");
}
template <bool PS, bool QS> struct KKStream {
  rsrc_t rp, rq; unsigned vp[4], vq[4];
  DI void init(int lane, int w, const void* p, unsigned pbytes, unsigned prow0, const void* q, unsigned qbytes, unsigned qrow0, unsigned ld) {
    rp = make_rsrc(p, pbytes); rq = make_rsrc(q, qbytes);
#pragma unroll
    for (int g = 0; g < 4; ++g) { const unsigned kv = kc_voff(lane, g & 1, ld); vp[g] = kv + (prow0 + (4 * w + g) * 8) * ld; vq[g] = kv + (qrow0 + (4 * w + g) * 8) * ld; }
  }
  DI void issue(unsigned slot0, int w, int t) {
    const unsigned sb = slot0 + w * 4096, so = t * 128;
    piece<16384, QS>(sb, vq[0], rq, so); piece<17408, QS>(sb, vq[1], rq, so); piece<18432, QS>(sb, vq[2], rq, so); piece<19456, QS>(sb, vq[3], rq, so);
    piece<0, PS>(sb, vp[0], rp, so); piece<1024, PS>(sb, vp[1], rp, so); piece<2048, PS>(sb, vp[2], rp, so); piece<3072, PS>(sb, vp[3], rp, so);
  }
};
struct T1Stream {
  rsrc_t rw, rx; unsigned vw[6], vx[4];
  DI void init(const Args& a, int lane, int w, unsigned s0, unsigned ct) {
    rw = make_rsrc(a.wqkv, 1024 * 3072 * 2); rx = make_rsrc(a.Ax, 2048 * 1024 * 2);
#pragma unroll
    for (int p = 0; p < 3; ++p)
#pragma unroll
      for (int g = 0; g < 2; ++g) vw[2 * p + g] = ic_voff(lane, g, 6144) + (2 * w + g) * 8 * 6144u + p * 2048u + ct * 128u;
#pragma unroll
    for (int g = 0; g < 4; ++g) vx[g] = kc_voff(lane, g & 1, 2048) + (s0 + (4 * w + g) * 8) * 2048u;
  }
  DI void issue(unsigned slot0, int w, int t) {
    const unsigned sw = slot0 + w * 2048, sx = slot0 + 24576 + w * 4096, ow = t * (64u * 6144u), ox = t * 128;
    piece<0, false>(sw, vw[0], rw, ow); piece<1024, false>(sw, vw[1], rw, ow); piece<8192, false>(sw, vw[2], rw, ow);
    piece<9216, false>(sw, vw[3], rw, ow); piece<16384, false>(sw, vw[4], rw, ow); piece<17408, false>(sw, vw[5], rw, ow);
    piece<0, false>(sx, vx[0], rx, ox); piece<1024, false>(sx, vx[1], rx, ox); piece<2048, false>(sx, vx[2], rx, ox); piece<3072, false>(sx, vx[3], rx, ox);
  }
};
struct KVStream {
  rsrc_t rk, rv; unsigned vk, vv;
  DI void init(const Args& a, int lane, int w, int head) {
    rk = make_rsrc(a.Q + (size_t)(32 + head) * 65536, 65536 * 2 + 4096); rv = make_rsrc(a.Q + (size_t)(64 + head) * 65536, 65536 * 2);
    vk = k_voff(lane) + 32 * 64 + w * 1024; vv = v_voff(lane) + w * 8 * 4096;
  }
  DI void issue(unsigned slot0, int w, int t) { const unsigned sb = slot0 + w * 1024; piece<32768, true>(sb, vk, rk, t * 4096); piece<36864, true>(sb, vv, rv, t * 128); }
};
struct T5Stream {
  rsrc_t rw, ro; unsigned vw[4], vo[2];
  DI void init(const Args& a, int lane, int w, unsigned n0, unsigned s5) {
    rw = make_rsrc(a.wo, 1024 * 1024 * 2); ro = make_rsrc(a.O, 2048 * 1024 * 2);
#pragma unroll
    for (int p = 0; p < 2; ++p)
#pragma unroll
      for (int g = 0; g < 2; ++g) vw[2 * p + g] = ic_voff(lane, g, 2048) + (2 * w + g) * 8 * 2048u + (n0 + p * 64) * 2u;
#pragma unroll
    for (int g = 0; g < 2; ++g) vo[g] = kc_voff(lane, g, 2048) + (s5 + (2 * w + g) * 8) * 2048u;
  }
  DI void issue(unsigned slot0, int w, int t) {
    const unsigned sb = slot0 + w * 2048, ow = t * (64u * 2048u), oo = t * 128;
    piece<0, false>(sb, vw[0], rw, ow); piece<1024, false>(sb, vw[1], rw, ow); piece<8192, false>(sb, vw[2], rw, ow); piece<9216, false>(sb, vw[3], rw, ow);
    piece<16384, true>(sb, vo[0], ro, oo); piece<17408, true>(sb, vo[1], ro, oo);
  }
};
struct Warm {
  rsrc_t rs; unsigned v, dv; int lo, hi, ph, extra;
  DI void step(int s) {
    const bool act = PF_ON && ((s >= lo && s < hi && ((s - lo) & 3) == ph) || s == extra);
    l2warm(v, rs, act ? 0u : OOB);
    if (act) v += dv;
  }
};
DI unsigned wl_x(int lane, int w, unsigned s0, int cl) { return (s0 + 32 * cl + (lane & 31)) * 2048u + ((lane >> 5) + 2 * w) * 128u; }
DI void warm_t1(Warm& f, const Args& a, int lane, int w, int rl, int cl, unsigned s0, unsigned ct) {
  const int wl = (w & 1) * 64 + lane;
  f.rs = w < 2 ? make_rsrc(a.Ax, 2048 * 1024 * 2) : make_rsrc(a.wqkv, 1024 * 3072 * 2);
  f.v = w < 2 ? wl_x(lane, w, s0, cl) : (wl < 96 ? (32 * rl + wl / 3) * 6144u + (wl % 3) * 2048u + ct * 128u : OOB);
  f.dv = w < 2 ? 512u : (wl < 96 ? 256u * 6144u : 0u);
}
constexpr __host__ __device__ int nwait(int s) { return npieces(s + 1) + npieces(s + 2) + 3; }
constexpr __host__ __device__ bool steady(int s0, int s1) { for (int s = s0; s < s1; ++s) if (nwait(s) != nwait(s0)) return false; return true; }
template <int S0, int S1, class F> DI void for_steps(F&& f) {
  if constexpr (S0 < S1) {
    if constexpr (steady(S0, S1) && S1 - S0 > 1) { for (int s = S0; s < S1; ++s) { wait_vm<nwait(S0)>(); f(s); } }
    else { wait_vm<nwait(S0)>(); f(S0); for_steps<S0 + 1, S1>(f); }
  }
}
DI void role_dma(const Args& a, const Ids& id) {
  const int lane = id.lane, w = id.wid - 8, xcd = id.xcd, idx = id.idx;
  const int rl = idx & 7, cl = idx >> 3, head = xcd * 4 + cl, half = xcd & 1, tb = idx & 3, eb = idx >> 2;
  const unsigned s0 = ((xcd & 1) * 8 + rl) * 128, ct = (xcd >> 1) * 4 + cl, eg = ct * 128;
  const unsigned t0 = (xcd >> 1) * 512 + tb * 128, e0 = eb * 128;
  const unsigned n0 = ((xcd & 1) * 4 + tb) * 128, s5 = ((xcd >> 1) * 8 + eb) * 64;
  const int ph1 = (rl + cl) & 3, ph4 = (tb + eb) & 3;
  auto slot_of = [&](int s) { return id.lds0 + (s & 3) * SLOT; };
  Warm f;
  T1Stream t1; t1.init(a, lane, w, s0, ct);
  warm_t1(f, a, lane, w, rl, cl, s0, ct);
  TLB(0);
  f.lo = 0; f.hi = 0; f.ph = 0; f.extra = -1; f.step(-1);
  t1.issue(slot_of(0), w, 0); t1.issue(slot_of(1), w, 1);
  wait_vm<10>(); barrier();
  TLB(1);
  t1.issue(slot_of(2), w, 2); t1.issue(slot_of(3), w, 3);
  f.extra = 0; f.step(0);
  f.lo = 0; f.hi = 8; f.ph = ph1; f.extra = -1; f.step(0);
  l2warm(OOB, f.rs, OOB);
  for_steps<1, 8>([&](int s) { barrier(); TLB(s + 1); t1.issue(slot_of(s + 3), w, s + 3); f.step(s); });
  const void* wd = eg >= 1024 ? a.Wi : a.Wr;
  f.rs = w < 2 ? make_rsrc(a.Bx, 2048 * 1024 * 2) : make_rsrc(wd, 1024 * 1024 * 2);
  f.v = w < 2 ? wl_x(lane, w, s0, cl) : (w == 2 ? ((eg & 1023) + 16 * rl + (lane & 15)) * 2048u + (lane >> 4) * 128u : OOB);
  f.dv = w < 3 ? 512u : 0u; f.lo = 8; f.hi = 24;
  for_steps<8, 13>([&](int s) { barrier(); TLB(s + 1); t1.issue(slot_of(s + 3), w, s + 3); f.step(s); });
  KKStream<false, false> g2; g2.init(lane, w, a.Bx, 2048 * 1024 * 2, s0, wd, 1024 * 1024 * 2, eg & 1023, 2048);
  for_steps<13, S_A - 3>([&](int s) { barrier(); TLB(s + 1); g2.issue(slot_of(s + 3), w, s + 3 - S_T2); f.step(s); });
  KVStream kv; kv.init(a, lane, w, head);
  Warm f2;
  if (w == 0) { f2.rs = make_rsrc(half ? a.Ci : a.Cr, 2048 * 2048 * 2); f2.v = (t0 + 16 * eb + (lane & 15)) * 4096u + (lane >> 4) * 128u; f2.dv = 512; f2.lo = S_T4 - 8; f2.hi = S_T4 + 24; f2.ph = ph4; f2.extra = -1; }
  else if (w < 3) { f2.rs = make_rsrc(a.ABt + (size_t)half * 1024 * 2048, 1024 * 2048 * 2);
    f2.v = (e0 + 32 * tb + (lane & 31)) * 4096u + ((lane >> 5) + 2 * (w - 1)) * 128u + 512; f2.dv = 512; f2.lo = S_T4; f2.hi = S_T4 + 24; f2.ph = ph4; f2.extra = S_T4 - 3; }
  else { f2.rs = make_rsrc(a.Q + (size_t)(32 + head) * 65536, 33 * 65536 * 2);
    f2.v = lane < 16 ? 2048u + (rl * 16 + lane) * 128u + 16384u : (lane < 32 ? 32u * 65536 * 2 + (rl * 4 + ((lane >> 2) & 3)) * 4096u + (lane & 3) * 128u + 512u : OOB);
    f2.dv = lane < 16 ? 16384u : (lane < 32 ? 512u : 0u); f2.lo = S_A; f2.hi = S_A + 24; f2.ph = idx & 3; f2.extra = S_A - 3; }
  for_steps<S_A - 3, S_A - 2>([&](int s) { if (w == 0) await(a.cnt, C_QKV + (head >> 1) * 2, 2, 32, lane);
    barrier(); TLB(s + 1); g2.issue(slot_of(s + 3), w, s + 3 - S_T2); kv.issue(slot_of(s + 3), w, s + 3 - S_A); if (w < 3) f.step(s); else f2.step(s); });
  f = f2;
  for_steps<S_A - 2, 29>([&](int s) { barrier(); TLB(s + 1); g2.issue(slot_of(s + 3), w, s + 3 - S_T2); kv.issue(slot_of(s + 3), w, s + 3 - S_A); f.step(s); });
  for_steps<29, S_T4 - 3>([&](int s) { barrier(); TLB(s + 1); kv.issue(slot_of(s + 3), w, s + 3 - S_A); f.step(s); });
  KKStream<true, false> g4; g4.init(lane, w, a.ABt + (size_t)half * 1024 * 2048, 1024 * 2048 * 2, e0, half ? a.Ci : a.Cr, 2048 * 2048 * 2, t0, 4096);
  for_steps<S_T4 - 3, S_T4 - 2>([&](int s) { if (w == 0) await(a.cnt, C_AB + (half * 8 + eb) * 2, 2, 32, lane);
    barrier(); TLB(s + 1); g4.issue(slot_of(s + 3), w, s + 3 - S_T4); kv.issue(slot_of(s + 3), w, s + 3 - S_A); f.step(s); });
  for_steps<S_T4 - 2, S_A + 29>([&](int s) { barrier(); TLB(s + 1); g4.issue(slot_of(s + 3), w, s + 3 - S_T4); kv.issue(slot_of(s + 3), w, s + 3 - S_A); f.step(s); });
  for_steps<S_A + 29, S_T5 - 8>([&](int s) { barrier(); TLB(s + 1); g4.issue(slot_of(s + 3), w, s + 3 - S_T4); f.step(s); });
  f.rs = make_rsrc(a.wo, 1024 * 1024 * 2); f.v = (128 * eb + (w & 1) * 64 + lane) * 2048u + n0 * 2 + (w >> 1) * 128u; f.dv = 0; f.lo = 0; f.hi = 0; f.extra = S_T5 - 8;
  for_steps<S_T5 - 8, S_T5 - 3>([&](int s) { barrier(); TLB(s + 1); g4.issue(slot_of(s + 3), w, s + 3 - S_T4); f.step(s); });
  T5Stream t5; t5.init(a, lane, w, n0, s5);
  for_steps<S_T5 - 3, S_T5 - 2>([&](int s) { if (w == 0) await(a.cnt, C_O + (s5 >> 8) * 4, 4, 32, lane);
    barrier(); TLB(s + 1); t5.issue(slot_of(s + 3), w, s + 3 - S_T5); f.step(s); });
  for_steps<S_T5 - 2, S_END - 3>([&](int s) { barrier(); TLB(s + 1); t5.issue(slot_of(s + 3), w, s + 3 - S_T5); f.step(s); });
  for_steps<S_END - 3, S_END>([&](int s) { barrier(); TLB(s + 1); f.step(s); });
}

DI void role_g0(const Args& a, const Ids& id) {
  const int wid = id.wid, wi = wid & 1, wj = wid >> 1;
  {
    const int lane = id.lane, rl = id.idx & 7, cl = id.idx >> 3, rb = (id.xcd & 1) * 8 + rl, ct = (id.xcd >> 1) * 4 + cl;
    const int s0 = rb * 128 + 64 * wj, head = 2 * ct + wi;
    Cons<32, 3, 2, true, 2> c;
#pragma unroll
    for (int i = 0; i < 3; ++i) c.pa[i] = id.lds0 + ic_raddr<32>(lane, 2 * i + wi);
#pragma unroll
    for (int k = 0; k < 4; ++k) c.qa[k] = id.lds0 + 24576 + kc_raddr<32>(lane, 64 * wj, k);
    c.zero();
    PlainHook hk;
    c.run<16, 4, SLOT>(hk, 0);
    const int ln = fresh_lane();
    const unsigned stg = id.lds0 + wj * SLOT + 32768 + wi * 4096;
    const rsrc_t rq = make_rsrc(a.Q, 3u * 32 * 65536 * 2);
    store_rows64<true>(c.acc, 0, QSCALE, stg, ln, rq, (unsigned)(head * 65536 + s0 * 32) * 2u, 64);
    barrier();
    store_rows64<true>(c.acc, 1, 1.0f, stg, ln, rq, (unsigned)((32 + head) * 65536 + s0 * 32) * 2u, 64);
    barrier();
    {
      const int dd = ln & 31, h = ln >> 5;
#pragma unroll
      for (int jb = 0; jb < 2; ++jb)
#pragma unroll
        for (int g = 0; g < 2; ++g) { const f32x16& v = c.acc[2][jb];
          lds_wr128(stg + dd * 128 + (((4 * jb + 2 * g + h) ^ (dd & 7)) * 16), u32x4{cvtpk(v[8 * g], v[8 * g + 1]), cvtpk(v[8 * g + 2], v[8 * g + 3]), cvtpk(v[8 * g + 4], v[8 * g + 5]), cvtpk(v[8 * g + 6], v[8 * g + 7])}); }
      flush128<true>(stg, ln, rq, (unsigned)((64 + head) * 65536 + s0) * 2u, 4096);
    }
    idle(2);
    signal(a.cnt, C_QKV + ct * 2 + (rb >> 3), ln);
    idle(S_A - 3 - 19);
  }
  {
    const int lane = fresh_lane(), r = lane & 31, h = lane >> 5;
    const int head = id.xcd * 4 + (id.idx >> 3), qb = id.idx & 7, q0 = qb * 256 + wid * 64;
    const rsrc_t rq = make_rsrc(a.Q + (size_t)head * 65536, 65536 * 2), rk = make_rsrc(a.Q + (size_t)(32 + head) * 65536, 65536 * 2);
    AttnWave w;
#pragma unroll
    for (int b = 0; b < 2; ++b)
#pragma unroll
      for (int s = 0; s < 2; ++s) w.q[b][s] = __builtin_bit_cast(bf16x8, ld16s(rq, (q0 + 32 * b + r) * 64 + 32 * s + 16 * h, 0));
    w.kf[0] = __builtin_bit_cast(bf16x8, ld16s(rk, r * 64 + 16 * h, 0)); w.kf[1] = __builtin_bit_cast(bf16x8, ld16s(rk, r * 64 + 32 + 16 * h, 0));
    idle(1);
#pragma unroll
    for (int b = 0; b < 2; ++b) {
      f32x16 s = mfma32(w.kf[0], w.q[b][0], f32x16{}); s = mfma32(w.kf[1], w.q[b][1], s);
      const float nm = -(rowmax16(s) + MARGIN);
#pragma unroll
      for (int i = 0; i < 16; ++i) w.NM[b][i] = nm;
      if (b == 0) w.S[0] = s + nm;
      w.O[b] = f32x16{}; w.L[b] = f32x4{};
    }
    w.S[1] = f32x16{};
    w.P[1][0] = u32x4{}; w.P[1][1] = u32x4{}; w.P[0][0] = u32x4{}; w.P[0][1] = u32x4{};
    w.vf[0] = bf16x8{}; w.vf[1] = bf16x8{};
    { const int m = lane & 15; const __bf16 one = (__bf16)1.0f, z = (__bf16)0.0f; const bool lo = (m == 0 || m == 4), hi = (m == 8 || m == 12);
      w.asel = bf16x8{lo ? one : z, lo ? one : z, lo ? one : z, lo ? one : z, hi ? one : z, hi ? one : z, hi ? one : z, hi ? one : z};
      w.sidx = (m == 4 || m == 12) ? 0xEEEE : 0x4444; }
    constexpr unsigned so = (S_A & 3) * SLOT;
    w.ka0 = id.lds0 + so + 32768 + k_raddr(lane, 0); w.ka1 = id.lds0 + so + 32768 + k_raddr(lane, 1);
    w.va0 = id.lds0 + so + 36864 + v_raddr(lane, 0); w.va1 = id.lds0 + so + 36864 + v_raddr(lane, 1);
    idle(1);
    w.run<4, SLOT>(S_A & 3);
    TLW(0, 100);
    const int ln = fresh_lane(), r2 = ln & 31, h2 = ln >> 5;
    const unsigned stg = id.lds0 + ((S_A + 32 + wj) & 3) * SLOT + 32768 + wi * 4096;
#pragma unroll
    for (int b = 0; b < 2; ++b) {
      const int src = (r2 & 15) + (r2 >> 4) * 32;
      float l = __shfl(w.L[b][0], src) + __shfl(w.L[b][0], src + 16);
      float amax = 0.f;
#pragma unroll
      for (int i = 0; i < 16; ++i) amax = __builtin_fmaxf(amax, __builtin_fabsf(w.O[b][i]));
      bool nan = false;
#pragma unroll
      for (int i = 0; i < 16; ++i) nan |= !(w.O[b][i] == w.O[b][i]);
      const bool bad = !(l < 0x1p126f) || !(amax < 0x1p126f) || nan || (amax < 0x1p-100f) || a.force_slow;
      if (__any(bad)) attn_slow(make_rsrc(a.Q + (size_t)(32 + head) * 65536, 65536 * 2), make_rsrc(a.Q + (size_t)(64 + head) * 65536, 65536 * 2), w.q[b], ln, w.O[b], l);
      const float inv = 1.0f / l;
      const f32x16 v = w.O[b] * inv;
      lds_wr128(stg + b * 2048 + r2 * 64 + ((h2 ^ ((r2 >> 1) & 3)) * 16), u32x4{cvtpk(v[0], v[1]), cvtpk(v[2], v[3]), cvtpk(v[4], v[5]), cvtpk(v[6], v[7])});
      lds_wr128(stg + b * 2048 + r2 * 64 + (((2 + h2) ^ ((r2 >> 1) & 3)) * 16), u32x4{cvtpk(v[8], v[9]), cvtpk(v[10], v[11]), cvtpk(v[12], v[13]), cvtpk(v[14], v[15])});
      barrier();
    }
    flush64<true>(stg, ln, make_rsrc(a.O, 2048 * 1024 * 2), (unsigned)(q0 * 1024 + head * 32) * 2u, 2048);
    idle(2);
    signal(a.cnt, C_O + qb * 4 + (id.xcd >> 1), ln);
    idle(S_T5 - (S_A + 36));
  }
  {
    const int lane = fresh_lane(), nl = id.idx & 3, sl = id.idx >> 2;
    const int n0 = ((id.xcd & 1) * 4 + nl) * 128, s5 = ((id.xcd >> 1) * 8 + sl) * 64;
    Cons<32, 2, 1, true> c;
    constexpr unsigned so = (S_T5 & 3) * SLOT;
#pragma unroll
    for (int i = 0; i < 2; ++i) c.pa[i] = id.lds0 + so + ic_raddr<32>(lane, 2 * wi + i);
#pragma unroll
    for (int k = 0; k < 4; ++k) c.qa[k] = id.lds0 + so + 16384 + kc_raddr<32>(lane, 32 * wj, k);
    c.zero();
    PlainHook hk;
    c.run<16, 4, SLOT>(hk, S_T5 & 3);
    const int ln = fresh_lane();
    store_rows128<false, 32>(c.acc, 0, 1.0f, id.lds0 + (2 + wj) * SLOT + 32768 + wi * 4096, ln, make_rsrc(a.out, 2048 * 2048 * 2), (unsigned)((s5 + 32 * wj) * 2048 + n0 + 64 * wi) * 2u, 4096);
  }
}

DI void role_g1(const Args& a, const Ids& id) {
  const int wi = id.wid & 1, wj = (id.wid >> 1) & 1, xcd = id.xcd, idx = id.idx;
  {
    const int lane = id.lane, rb = (xcd & 1) * 8 + (idx & 7), ct = (xcd >> 1) * 4 + (idx >> 3);
    const int s0 = rb * 128, eg = ct * 128;
    constexpr int SH = T2_SH, NB = 64 / SH;
    Cons<SH, NB, NB, false> c;
#pragma unroll
    for (int k = 0; k < c.NKS; ++k) { c.pa[k] = id.lds0 + kc_raddr<SH>(lane, 64 * wi, k); c.qa[k] = id.lds0 + 16384 + kc_raddr<SH>(lane, 64 * wj, k); }
    c.zero();
    idle(16);
    PlainHook hk;
    c.template run<16, 4, SLOT>(hk, 0);
    const int ln = fresh_lane();
    const unsigned stg = id.lds0 + (id.wid & 3) * 4096;
    const float sc = eg >= 1024 ? -1.0f : 1.0f;
    const rsrc_t rab = make_rsrc(a.ABt, 2048u * 2048 * 2);
    store_rows128<true, SH>(c.acc, 0, sc, stg, ln, rab, (unsigned)((eg + 64 * wj) * 2048 + s0 + 64 * wi) * 2u, 4096);
    barrier();
    store_rows128<true, SH>(c.acc, 1, sc, stg, ln, rab, (unsigned)((eg + 64 * wj + 32) * 2048 + s0 + 64 * wi) * 2u, 4096);
    idle(2);
    signal(a.cnt, C_AB + ct * 2 + (rb >> 3), ln);
    idle(S_T4 - 35);
  }
  {
    const int lane = fresh_lane(), half = xcd & 1, t0 = (xcd >> 1) * 512 + (idx & 3) * 128, e0 = (idx >> 2) * 128;
    constexpr int SH = G_SH, NB = 64 / SH;
    Cons<SH, NB, NB, false> c;
    constexpr unsigned so = (S_T4 & 3) * SLOT;
#pragma unroll
    for (int k = 0; k < c.NKS; ++k) { c.pa[k] = id.lds0 + so + kc_raddr<SH>(lane, 64 * wi, k); c.qa[k] = id.lds0 + so + 16384 + kc_raddr<SH>(lane, 64 * wj, k); }
    c.zero();
    PlainHook hk;
    c.template run<32, 4, SLOT>(hk, S_T4 & 3);
    TLW(4, 101);
    const int ln = fresh_lane();
    constexpr int NR = SH == 16 ? 16 : 4, NV = SH == 16 ? 1 : 4;
    if (wj != half) {
      const rsrc_t rx = make_rsrc(a.X + (size_t)(blockIdx.x * 2 + wi) * 4096, 16384);
#pragma unroll
      for (int m = 0; m < NR; ++m)
#pragma unroll
        for (int v = 0; v < NV; ++v) { const auto& t = c.acc[m / NB][m % NB];
          st16s(rx, ln * 16, (m * NV + v) * 1024, __builtin_bit_cast(u32x4, f32x4{t[4 * v], t[4 * v + 1], t[4 * v + 2], t[4 * v + 3]})); }
      idle(2);
      signal(a.cnt, C_P + (blockIdx.x ^ 1) * 2 + wi, ln);
      return;
    }
    idle(7);
    await(a.cnt, C_P + blockIdx.x * 2 + wi, 1, 1, ln);
    { const rsrc_t rx = make_rsrc(a.X + (size_t)((blockIdx.x ^ 1) * 2 + wi) * 4096, 16384);
      u32x4 y[16];
#pragma unroll
      for (int m = 0; m < 16; ++m) y[m] = ld16s(rx, ln * 16, m * 1024);
      idle(2);
#pragma unroll
      for (int m = 0; m < NR; ++m)
#pragma unroll
        for (int v = 0; v < NV; ++v) { auto& t = c.acc[m / NB][m % NB]; const f32x4 u = __builtin_bit_cast(f32x4, y[m * NV + v]);
          t[4 * v] += u[0]; t[4 * v + 1] += u[1]; t[4 * v + 2] += u[2]; t[4 * v + 3] += u[3]; } }
    const unsigned stg = id.lds0 + 32768 + wi * 4096;
    const rsrc_t ro = make_rsrc(a.out, 2048 * 2048 * 2);
#pragma unroll
    for (int p = 0; p < 2; ++p)
      store_rows128<false, SH>(c.acc, p, 1.0f, stg, ln, ro, (unsigned)((t0 + 64 * wj + 32 * p) * 2048 + 1024 + e0 + 64 * wi) * 2u, 4096);
  }
}

DI Ids make_ids(char* lds) { Ids id; id.lane = threadIdx.x & 63; id.wid = __builtin_amdgcn_readfirstlane(threadIdx.x >> 6); id.xcd = blockIdx.x & 7; id.idx = blockIdx.x >> 3; id.lds0 = (unsigned)(size_t)(LAS char*)lds; return id; }
__global__ __launch_bounds__(768, 3) void mega_kernel(Args a) {
  extern __shared__ __attribute__((aligned(16))) char lds[];
  const Ids id = make_ids(lds);
  if (id.wid < 4) { role_g0(a, id); TLW(0, 102); }
  else if (id.wid < 8) { role_g1(a, id); }
  else role_dma(a, id);
}
__global__ __launch_bounds__(64) void init_kernel(Args a) {
  Ids id; id.lane = threadIdx.x; id.wid = 0; id.xcd = blockIdx.x & 7; id.idx = blockIdx.x >> 3; id.lds0 = 0;
  const int i = blockIdx.x * 64 + threadIdx.x;
  if (i < C_LINES * 8) ((u32x4*)a.cnt)[i] = u32x4{0, 0, 0, 0};
  if (PF_ON) {
    const int rl = id.idx & 7, cl = id.idx >> 3; const unsigned s0 = ((id.xcd & 1) * 8 + rl) * 128, ct = (id.xcd >> 1) * 4 + cl;
#pragma unroll
    for (int w = 0; w < 4; ++w) { Warm f; warm_t1(f, a, id.lane, w, rl, cl, s0, ct); f.lo = 0; f.hi = 0; f.ph = 0; f.extra = 0; f.step(0); f.step(0); }
  }
}

extern "C" void kernel_launch(void* const* d_in, const int* in_sizes, int n_in, void* d_out, int out_size, void* d_ws, size_t ws_size, hipStream_t stream) {
  u16* ws = (u16*)d_ws; const size_t M = (size_t)2048 * 1024;
  Args a;
  a.Ax = (const u16*)d_in[0]; a.wqkv = (const u16*)d_in[1]; a.wo = (const u16*)d_in[2]; a.Bx = (const u16*)d_in[3];
  a.Cr = (const u16*)d_in[4]; a.Ci = (const u16*)d_in[5]; a.Wr = (const u16*)d_in[6]; a.Wi = (const u16*)d_in[7];
  a.Q = ws; a.ABt = ws + 3 * M; a.O = ws + 5 * M; a.X = (float*)(ws + 6 * M); a.out = (u16*)d_out;
  a.cnt = (unsigned*)(ws + 12 * M); a.dbg = nullptr; a.force_slow = 0; a.mode = 0;
  (void)hipFuncSetAttribute((const void*)mega_kernel, hipFuncAttributeMaxDynamicSharedMemorySize, LDS_ALL);
  init_kernel<<<256, 64, 0, stream>>>(a);
  mega_kernel<<<256, 768, LDS_ALL, stream>>>(a);
}
```

```cpp
#include <hip/hip_runtime.h>
typedef unsigned short u16;
typedef __bf16 bf16x8 __attribute__((ext_vector_type(8)));
typedef __bf16 bf16x16 __attribute__((ext_vector_type(16)));
typedef __bf16 bf16x2 __attribute__((ext_vector_type(2)));
typedef short s16x4 __attribute__((ext_vector_type(4)));
typedef short s16x8 __attribute__((ext_vector_type(8)));
typedef float f32x2 __attribute__((ext_vector_type(2)));
typedef float f32x4 __attribute__((ext_vector_type(4)));
typedef float f32x16 __attribute__((ext_vector_type(16)));
typedef unsigned u32x4 __attribute__((ext_vector_type(4)));
typedef unsigned u32x2 __attribute__((ext_vector_type(2)));
#define LAS __attribute__((address_space(3)))
#define DI __device__ __forceinline__
#define SBAR() __builtin_amdgcn_sched_barrier(0)
#define PIN(x) asm volatile("" : "+v"(x))
typedef __amdgpu_buffer_rsrc_t rsrc_t;

DI int fresh_lane() { int l; asm volatile("v_mbcnt_lo_u32_b32 %0, -1, 0\n\tv_mbcnt_hi_u32_b32 %0, -1, %0" : "=v"(l)); return l; }
DI rsrc_t make_rsrc(const void* p, unsigned bytes) { return __builtin_amdgcn_make_buffer_rsrc((void*)p, 0, bytes, 0x00020000); }
DI void dma16(unsigned ldsaddr, unsigned voff, rsrc_t rs, unsigned soff) {
  asm volatile("s_mov_b32 m0, %0\n\ts_nop 0\n\tbuffer_load_dwordx4 %1, %2, %3 offen lds" ::"s"(ldsaddr), "v"(voff), "s"(rs), "s"(soff) : "memory");
}
DI void dma16s(unsigned ldsaddr, unsigned voff, rsrc_t rs, unsigned soff) {
  asm volatile("s_mov_b32 m0, %0\n\ts_nop 0\n\tbuffer_load_dwordx4 %1, %2, %3 offen sc1 lds" ::"s"(ldsaddr), "v"(voff), "s"(rs), "s"(soff) : "memory");
}
DI u32x4 ld16s(rsrc_t rs, unsigned voff, unsigned soff) { return __builtin_amdgcn_raw_buffer_load_b128(rs, voff, soff, 16); }
DI void st16s(rsrc_t rs, unsigned voff, unsigned soff, u32x4 v) { __builtin_amdgcn_raw_buffer_store_b128(v, rs, voff, soff, 16); }
DI void l2warm(unsigned voff, rsrc_t rs, unsigned soff) {
  asm volatile("buffer_load_dword v167, %0, %1, %2 offen sc1" ::"v"(voff), "s"(rs), "s"(soff) : "memory", "v167");
}
constexpr unsigned OOB = 0x7ff00000u;
template <int N> DI void wait_vm() { asm volatile("s_waitcnt vmcnt(%0)" ::"n"(N) : "memory"); }
DI void wait_lgkm0() { asm volatile("s_waitcnt lgkmcnt(0)" ::: "memory"); }
DI void barrier() { asm volatile("s_barrier" ::: "memory"); }
DI unsigned cvtpk(float lo, float hi) { f32x2 v = {lo, hi}; bf16x2 b = __builtin_convertvector(v, bf16x2); return __builtin_bit_cast(unsigned, b); }
DI f32x16 mfma32(bf16x8 a, bf16x8 b, f32x16 c) { return __builtin_amdgcn_mfma_f32_32x32x16_bf16(a, b, c, 0, 0, 0); }
DI f32x4 mfma16(bf16x8 a, bf16x8 b, f32x4 c) { return __builtin_amdgcn_mfma_f32_16x16x32_bf16(a, b, c, 0, 0, 0); }
DI bf16x8 lds_rd128(unsigned addr) { return *(const LAS bf16x8*)(size_t)addr; }
DI u32x4 lds_rdu(unsigned addr) { return *(const LAS u32x4*)(size_t)addr; }
DI void lds_wr64(unsigned addr, u32x2 v) { *(LAS u32x2*)(size_t)addr = v; }
DI void lds_wr128(unsigned addr, u32x4 v) { *(LAS u32x4*)(size_t)addr = v; }
DI s16x4 lds_rdtr(unsigned addr) { return __builtin_amdgcn_ds_read_tr16_b64_v4i16((LAS s16x4*)(size_t)addr); }
DI bf16x8 cat8(s16x4 a, s16x4 b) { s16x8 r = __builtin_shufflevector(a, b, 0, 1, 2, 3, 4, 5, 6, 7); return __builtin_bit_cast(bf16x8, r); }

DI unsigned kc_voff(int lane, int par, unsigned ldb) { return (unsigned)(lane >> 3) * ldb + (unsigned)(((lane & 7) ^ (((lane >> 4) + 4 * par) & 7)) * 16); }
DI unsigned ic_voff(int lane, int par, unsigned ldb) { const int c = lane & 7, u = (c >> 1) ^ (((lane >> 4) & 1) << 1) ^ par; return (unsigned)(lane >> 3) * ldb + (unsigned)((u * 2 + (c & 1)) * 16); }
template <int SH> DI unsigned kc_raddr(int lane, int row0, int ks) {
  const int row = row0 + (lane & (SH - 1)), ch = SH == 16 ? 4 * ks + (lane >> 4) : 2 * ks + (lane >> 5);
  return (unsigned)(row * 128 + ((ch ^ ((row >> 1) & 7)) * 16)); }
template <int SH> DI unsigned ic_raddr(int lane, int cb) {
  const int g = lane >> 4, q = (lane & 15) >> 2, p = lane & 3;
  const int kg = SH == 16 ? g : (g >> 1), c16 = SH == 16 ? cb : 2 * cb + (g & 1);
  const int m = (((q >> 1) & 1) << 1) ^ (kg & 1);
  return (unsigned)((c16 >> 2) * 8192 + (8 * kg + q) * 128 + (((c16 & 3) ^ m) * 32) + p * 8);
}
template <int SH> struct AccT; template <> struct AccT<16> { typedef f32x4 t; }; template <> struct AccT<32> { typedef f32x16 t; };

template <int SH, int NI, int NJ, bool PIC, int SWAPI = -1> struct Cons {
  static constexpr int NKS = SH == 16 ? 2 : 4, NPA = PIC ? NI : NKS, NM = NI * NJ, BLK = SH * 128, KSO = SH == 16 ? 4096 : 2048;
  typedef typename AccT<SH>::t acc_t;
  unsigned pa[NPA], qa[NKS];
  acc_t acc[NI][NJ];
  bf16x8 p0[NI], q0[NJ], p1[NI], q1[NJ];
  static constexpr int PI = PIC ? 2 : 1, NIT = NJ + NI * PI;
  s16x4 tlo;
  template <int KS, int K> DI void rd(bf16x8 (&p)[NI], bf16x8 (&q)[NJ]) {
    if constexpr (K == 0) q[0] = lds_rd128(qa[KS]);
    else if constexpr (K > PI && K < PI + NJ) q[K - PI] = lds_rd128(qa[KS] + (K - PI) * BLK);
    else { constexpr int r = K <= PI ? K - 1 : K - NJ, i = r / PI, hf = r % PI;
      if constexpr (!PIC) p[i] = lds_rd128(pa[KS] + i * BLK);
      else if constexpr (hf == 0) tlo = lds_rdtr(pa[i] + KS * KSO);
      else p[i] = cat8(tlo, lds_rdtr(pa[i] + KS * KSO + 512)); }
  }
  template <int I, int J> DI void mma(bf16x8 a, bf16x8 b) {
    if constexpr (SH == 16) acc[I][J] = mfma16(a, b, acc[I][J]);
    else if constexpr (I == SWAPI) acc[I][J] = mfma32(b, a, acc[I][J]);
    else acc[I][J] = mfma32(a, b, acc[I][J]); }
  template <int KS, bool RD, int G0, bool FIRST, int M, class H> DI void grp(bf16x8 (&cp)[NI], bf16x8 (&cq)[NJ], bf16x8 (&np)[NI], bf16x8 (&nq)[NJ], H& hk, int delta) {
    if constexpr (M < NM) {
      mma<M / NJ, M % NJ>(cp[M / NJ], cq[M % NJ]); SBAR();
      if constexpr (FIRST && M == 0) { advance(delta); hk.sync(); SBAR(); }
      constexpr int RG = NM - 1 < NIT ? (NM > 1 ? NM - 1 : 1) : NIT;
      constexpr int lo = (M * NIT + RG - 1) / RG, hi = M + 1 < RG ? ((M + 1) * NIT + RG - 1) / RG : NIT;
      if constexpr (RD && M < RG) {
        if constexpr (lo < hi) rd<KS, lo>(np, nq);
        if constexpr (lo + 1 < hi) rd<KS, lo + 1>(np, nq);
        if constexpr (lo + 2 < hi) rd<KS, lo + 2>(np, nq);
        if constexpr (lo + 3 < hi) rd<KS, lo + 3>(np, nq);
        if constexpr (lo + 4 < hi) rd<KS, lo + 4>(np, nq);
      }
      hk.template at<G0 + M>();
      SBAR();
      grp<KS, RD, G0, FIRST, M + 1>(cp, cq, np, nq, hk, delta);
    }
  }
  DI void advance(int delta) {
#pragma unroll
    for (int i = 0; i < NPA; ++i) { pa[i] += delta; PIN(pa[i]); }
#pragma unroll
    for (int i = 0; i < NKS; ++i) { qa[i] += delta; PIN(qa[i]); }
  }
  template <int G, class H> DI void group(H& hk, int delta = 0) {
    if constexpr ((G & 1) == 0) grp<G, true, G * NM, G == 0, 0>(p1, q1, p0, q0, hk, delta);
    else grp<G, true, G * NM, false, 0>(p0, q0, p1, q1, hk, 0);
  }
  template <class H> DI void tail(H& hk) { grp<0, false, 1000, false, 0>(p1, q1, p0, q0, hk, 0); }
  template <int NT, int NS, int SLOT, class H> DI void run(H& hk, int s0) {
    int slot = s0;
    group<0>(hk, 0);
    if constexpr (NKS == 4) group<1>(hk);
    for (int t = 1; t < NT; ++t) {
      const int d = slot == NS - 1 ? -(NS - 1) * SLOT : SLOT; slot = slot == NS - 1 ? 0 : slot + 1;
      if constexpr (NKS == 4) { group<2>(hk); group<3>(hk); group<0>(hk, d); group<1>(hk); }
      else { group<1>(hk); group<0>(hk, d); }
    }
    if constexpr (NKS == 4) { group<2>(hk); group<3>(hk); } else group<1>(hk);
    tail(hk);
  }
  DI void zero() {
#pragma unroll
    for (int i = 0; i < NI; ++i)
#pragma unroll
      for (int j = 0; j < NJ; ++j) acc[i][j] = acc_t{};
#pragma unroll
    for (int i = 0; i < NI; ++i) p1[i] = bf16x8{};
#pragma unroll
    for (int j = 0; j < NJ; ++j) q1[j] = bf16x8{};
  }
};
struct PlainHook { template <int M> DI void at() {} DI void sync() { asm volatile("s_waitcnt lgkmcnt(0)\n\ts_barrier" ::: "memory"); } };
struct NoSyncHook { template <int M> DI void at() {} DI void sync() { asm volatile("s_waitcnt lgkmcnt(0)" ::: "memory"); } };

DI u32x2 pk4(float a, float b, float c, float d, float sc) { return u32x2{cvtpk(a * sc, b * sc), cvtpk(c * sc, d * sc)}; }
template <bool SC1> DI void st16(rsrc_t rs, unsigned voff, unsigned soff, u32x4 v) { __builtin_amdgcn_raw_buffer_store_b128(v, rs, voff, soff, SC1 ? 16 : 0); }
template <bool SC1> DI void flush64(unsigned stg, int lane, rsrc_t rs, unsigned soff, unsigned ldb) {
#pragma unroll
  for (int m = 0; m < 4; ++m) { const int row = 16 * m + (lane >> 2), c = lane & 3;
    st16<SC1>(rs, row * ldb + c * 16, soff, lds_rdu(stg + row * 64 + ((c ^ ((row >> 1) & 3)) * 16))); }
}
template <bool SC1> DI void flush128(unsigned stg, int lane, rsrc_t rs, unsigned soff, unsigned ldb) {
#pragma unroll
  for (int m = 0; m < 4; ++m) { const int row = 8 * m + (lane >> 3), c = lane & 7;
    st16<SC1>(rs, row * ldb + c * 16, soff, lds_rdu(stg + row * 128 + ((c ^ (row & 7)) * 16))); }
}
template <bool SCALE> DI void prep_tile64(const f32x16& v, float sc, unsigned stg, int lane, u32x4* r) {
  const int row = lane & 31, h = lane >> 5;
#pragma unroll
  for (int t = 0; t < 4; ++t) {
    const u32x2 w = SCALE ? pk4(v[4 * t], v[4 * t + 1], v[4 * t + 2], v[4 * t + 3], sc) : u32x2{cvtpk(v[4 * t], v[4 * t + 1]), cvtpk(v[4 * t + 2], v[4 * t + 3])};
    lds_wr64(stg + row * 64 + ((t ^ ((row >> 1) & 3)) * 16) + h * 8, w); }
#pragma unroll
  for (int m = 0; m < 2; ++m) { const int rr = 16 * m + (lane >> 2), c = lane & 3; r[m] = lds_rdu(stg + rr * 64 + ((c ^ ((rr >> 1) & 3)) * 16)); }
}
template <bool SC1> DI void commit64(const u32x4* r, int lane, rsrc_t rs, unsigned soff, unsigned ldb) {
#pragma unroll
  for (int m = 0; m < 2; ++m) st16<SC1>(rs, (16 * m + (lane >> 2)) * ldb + (lane & 3) * 16, soff, r[m]);
}
DI void read128(unsigned stg, int lane, u32x4* r) {
#pragma unroll
  for (int m = 0; m < 4; ++m) { const int row = 8 * m + (lane >> 3), c = lane & 7; r[m] = lds_rdu(stg + row * 128 + ((c ^ (row & 7)) * 16)); }
}
template <bool SC1> DI void commit128(const u32x4* r, int lane, rsrc_t rs, unsigned soff, unsigned ldb) {
#pragma unroll
  for (int m = 0; m < 4; ++m) st16<SC1>(rs, (8 * m + (lane >> 3)) * ldb + (lane & 7) * 16, soff, r[m]);
}
DI u32x2 pk4x(float a, float b, float c, float d, unsigned sgn) { return u32x2{cvtpk(a, b) ^ sgn, cvtpk(c, d) ^ sgn}; }
template <int SH, int NI, int NJ, class A> DI void prep_rows128(const A (&acc)[NI][NJ], int j32, unsigned sgn, unsigned stg, int lane, u32x4* r) {
  if constexpr (SH == 16) {
    const int j = lane & 15, g = lane >> 4;
#pragma unroll
    for (int jj = 0; jj < 2; ++jj)
#pragma unroll
      for (int ii = 0; ii < 4; ++ii) { const A& v = acc[ii][2 * j32 + jj]; const int row = 16 * jj + j;
        lds_wr64(stg + row * 128 + (((2 * ii + (g >> 1)) ^ (row & 7)) * 16) + (g & 1) * 8, pk4x(v[0], v[1], v[2], v[3], sgn)); }
  } else {
    const int row = lane & 31, h = lane >> 5;
#pragma unroll
    for (int ii = 0; ii < 2; ++ii)
#pragma unroll
      for (int t = 0; t < 4; ++t) { const A& v = acc[ii][j32];
        lds_wr64(stg + row * 128 + (((4 * ii + t) ^ (row & 7)) * 16) + h * 8, pk4x(v[4 * t], v[4 * t + 1], v[4 * t + 2], v[4 * t + 3], sgn)); }
  }
  read128(stg, lane, r);
}
template <bool SC1, int SH, int NI, int NJ, class A> DI void store_rows128(const A (&acc)[NI][NJ], int j32, unsigned sgn, unsigned stg, int lane, rsrc_t rs, unsigned soff, unsigned ldb) {
  u32x4 r[4]; prep_rows128<SH>(acc, j32, sgn, stg, lane, r); commit128<SC1>(r, lane, rs, soff, ldb);
}
#define STAMP()
#define STAMP_END(W)

DI float ex2(float x) { return __builtin_amdgcn_exp2f(x); }
DI bf16x16 cat16(u32x4 a, u32x4 b) { typedef unsigned u32x8 __attribute__((ext_vector_type(8))); u32x8 r = __builtin_shufflevector(a, b, 0, 1, 2, 3, 4, 5, 6, 7); return __builtin_bit_cast(bf16x16, r); }
DI int iperm(int r) { const int b = r & 3, h = (r >> 2) & 1, t = r >> 3; return b + 4 * (t & 1) + 8 * h + 16 * (t >> 1); }
DI unsigned k_voff(int lane) { return (unsigned)((lane >> 2) * 64 + (((lane & 3) ^ ((lane >> 4) & 3)) * 16)); }
DI unsigned v_voff(int lane) { const int c = lane & 7; return (unsigned)((lane >> 3) * 4096 + ((c & 4) | ((c & 3) ^ ((lane >> 4) & 3))) * 16); }
DI unsigned k_raddr(int lane, int s) { const int r = lane & 31, h = lane >> 5; return (unsigned)(r * 64 + (((2 * s + h) ^ ((r >> 2) & 3)) * 16)); }
DI unsigned v_raddr(int lane, int s) { const int d = iperm(lane & 31), h = lane >> 5; return (unsigned)(d * 128 + (((2 * s + h) ^ ((d >> 1) & 3)) * 16)); }
DI float swap32_max(float m) { auto rr = __builtin_amdgcn_permlane32_swap(__float_as_uint(m), __float_as_uint(m), false, false); return __builtin_fmaxf(__uint_as_float(rr[0]), __uint_as_float(rr[1])); }
DI float swap32_sum(float m) { auto rr = __builtin_amdgcn_permlane32_swap(__float_as_uint(m), __float_as_uint(m), false, false); return __uint_as_float(rr[0]) + __uint_as_float(rr[1]); }
DI float rowmax16(const f32x16& s) { float m = s[0];
#pragma unroll
  for (int i = 1; i < 16; ++i) m = __builtin_fmaxf(m, s[i]);
  return swap32_max(m); }
constexpr float MARGIN = 100.0f;

struct AttnWave {
  f32x16 S[2], NM[2], O[2]; f32x4 L[2];
  u32x4 P[2][2];
  bf16x8 q[2][2], kf[2], vf[2];
  bf16x8 asel; int sidx;
  unsigned ka0, ka1, va0, va1;
  template <int X> DI void exp4(int i) { S[X][i] = ex2(S[X][i]); S[X][i + 1] = ex2(S[X][i + 1]); S[X][i + 2] = ex2(S[X][i + 2]); S[X][i + 3] = ex2(S[X][i + 3]); }
  template <int X> DI void cvt2(int i) { P[X][i >> 3][(i >> 1) & 3] = cvtpk(S[X][i], S[X][i + 1]); P[X][i >> 3][((i >> 1) & 3) + 1] = cvtpk(S[X][i + 2], S[X][i + 3]); }
#define BARPOS 1
#define AWAIT 0
  DI void sync(int delta) { ka0 += delta; ka1 += delta; va0 += delta; va1 += delta; PIN(ka0); PIN(ka1); PIN(va0); PIN(va1); asm volatile("s_waitcnt lgkmcnt(0)\n\ts_barrier" ::: "memory"); SBAR(); }
  template <int X, int RD, bool AFTER_RD = false> DI void slot(int delta = 0) {
    constexpr int Y = X ^ 1, B = RD - 1;
    constexpr int BP = RD == 1 ? BARPOS : -1;
    if constexpr (BP == 0) sync(delta);
    if constexpr (AWAIT && AFTER_RD) __builtin_amdgcn_s_waitcnt(0xC27F);
    S[Y] = mfma32(kf[0], q[Y][0], NM[Y]); SBAR();
    if constexpr (BP == 1) sync(delta);
    if constexpr (RD && BP <= 1) { kf[0] = lds_rd128(ka0 + B * 2048); SBAR(); }
    exp4<X>(0); PIN(S[X]); SBAR();
    S[Y] = mfma32(kf[1], q[Y][1], S[Y]); SBAR();
    if constexpr (BP == 2) { sync(delta); kf[0] = lds_rd128(ka0 + B * 2048); SBAR(); }
    if constexpr (RD && BP <= 2) { kf[1] = lds_rd128(ka1 + B * 2048); SBAR(); }
    exp4<X>(4); cvt2<X>(0); PIN(S[X]); PIN(P[X][0]); SBAR();
    if constexpr (AWAIT && AFTER_RD) __builtin_amdgcn_s_waitcnt(0xC07F);
    O[Y] = mfma32(vf[0], __builtin_bit_cast(bf16x8, P[Y][0]), O[Y]); SBAR();
    if constexpr (BP == 3) { sync(delta); kf[0] = lds_rd128(ka0 + B * 2048); kf[1] = lds_rd128(ka1 + B * 2048); SBAR(); }
    if constexpr (RD) { vf[0] = lds_rd128(va0 + B * 64); SBAR(); }
    exp4<X>(8); cvt2<X>(4); PIN(S[X]); PIN(P[X][0]); SBAR();
    O[Y] = mfma32(vf[1], __builtin_bit_cast(bf16x8, P[Y][1]), O[Y]); SBAR();
    if constexpr (RD) { vf[1] = lds_rd128(va1 + B * 64); SBAR(); }
    exp4<X>(12); cvt2<X>(8); PIN(S[X]); PIN(P[X][1]); SBAR();
    L[Y] = __builtin_amdgcn_smfmac_f32_16x16x64_bf16(asel, cat16(P[Y][0], P[Y][1]), L[Y], sidx, 0, 0); SBAR();
    cvt2<X>(12); PIN(P[X][1]); SBAR();
  }
  template <int NS, int SLOT> DI void run(int sl) {
    slot<0, 1>(0); slot<1, 0, true>();
    for (int t = 1; t < 32; ++t) {
      const int d = sl == NS - 1 ? -(NS - 1) * SLOT : SLOT; sl = sl == NS - 1 ? 0 : sl + 1;
      slot<0, 2>(); slot<1, 0, true>(); slot<0, 1>(d); slot<1, 0, true>();
    }
    slot<0, 2>(); slot<1, 0, true>();
    O[1] = mfma32(vf[0], __builtin_bit_cast(bf16x8, P[1][0]), O[1]);
    O[1] = mfma32(vf[1], __builtin_bit_cast(bf16x8, P[1][1]), O[1]);
    L[1] = __builtin_amdgcn_smfmac_f32_16x16x64_bf16(asel, cat16(P[1][0], P[1][1]), L[1], sidx, 0, 0);
  }
};

DI void attn_slow(rsrc_t rk, rsrc_t rv, const bf16x8 (&q)[2], int lane, f32x16& O, float& l) {
  const int r = lane & 31, h = lane >> 5;
  float m = -INFINITY; l = 0.f; O = f32x16{};
  const unsigned ko = r * 64 + h * 16, vo = iperm(r) * 4096 + h * 16;
  for (int j = 0; j < 64; ++j) {
    f32x16 s = mfma32(__builtin_bit_cast(bf16x8, ld16s(rk, ko, j * 2048)), q[0], f32x16{});
    s = mfma32(__builtin_bit_cast(bf16x8, ld16s(rk, ko + 32, j * 2048)), q[1], s);
    const float mn = __builtin_fmaxf(m, rowmax16(s));
    const float f = ex2(m - mn); m = mn;
    float ps = 0.f; u32x4 p0, p1;
#pragma unroll
    for (int i = 0; i < 16; ++i) s[i] = ex2(s[i] - mn);
#pragma unroll
    for (int i = 0; i < 4; ++i) { p0[i] = cvtpk(s[2 * i], s[2 * i + 1]); p1[i] = cvtpk(s[8 + 2 * i], s[9 + 2 * i]); }
#pragma unroll
    for (int i = 0; i < 4; ++i) { ps += __uint_as_float(p0[i] << 16) + __uint_as_float(p0[i] & 0xffff0000u) + __uint_as_float(p1[i] << 16) + __uint_as_float(p1[i] & 0xffff0000u); }
    l = l * f + swap32_sum(ps);
    O = O * f;
    O = mfma32(__builtin_bit_cast(bf16x8, ld16s(rv, vo, j * 64)), __builtin_bit_cast(bf16x8, p0), O);
    O = mfma32(__builtin_bit_cast(bf16x8, ld16s(rv, vo + 32, j * 64)), __builtin_bit_cast(bf16x8, p1), O);
  }
}

constexpr float QSCALE = 0.17677669529663687f * 1.4426950408889634f;
constexpr int SLOT = 40960, LDS_ALL = 163840;
#define S_A 27
#define S_T4 40
#define T2PF 1
#define QKV_DRAIN 1
#define O_DRAIN 1
#define AB_DRAIN 1
constexpr int S_WO = S_T4 + 16;
constexpr int S_T2 = 16, S_T5 = S_T4 + 32, S_END = S_T5 + 16;
static_assert(S_A >= 25 && S_A <= 28 && S_T5 - 3 - (S_A + 35 + O_DRAIN) >= 0 && S_A - 3 - 20 - QKV_DRAIN >= 0 && S_T4 - 3 - 35 - AB_DRAIN >= 0 && S_WO <= S_A + 31 && S_WO >= S_A + 29 && S_T4 >= 39 && S_T4 <= S_A + 14, "schedule");
#define T2_SH 32
#define G_SH 16
#define PF_ON 1
struct Args {
  const u16 *Ax, *wqkv, *wo, *Bx, *Cr, *Ci, *Wr, *Wi;
  u16 *Q, *ABt, *O; float* X; u16* out;
  unsigned long long* flg;
  unsigned long long* dbg; int force_slow; int mode;
};
constexpr int F_QKV = 0, F_AB = 1024, F_O = 2048, F_P = 3072, F_EPOCH = 3584 + 16, F_TMO = 3584 + 48, F_WORDS = 3584 + 64;
struct Ids { int lane, wid, xcd, idx; unsigned lds0; };
#define TL_DECL
#define TLB(t)
#define TLW(w, t)
#define TL_DUMP(w, slot)
DI void idle(int n) { for (int i = 0; i < n; ++i) barrier(); }
typedef unsigned long long u64;
DI u64 load_epoch(const Args& a) { return __hip_atomic_load(a.flg + F_EPOCH, __ATOMIC_RELAXED, __HIP_MEMORY_SCOPE_AGENT) + 1; }
DI void signal(u64* flg, int word, u64 e, int lane) { wait_vm<0>(); if (lane == 0) __hip_atomic_store(flg + word, e, __ATOMIC_RELAXED, __HIP_MEMORY_SCOPE_AGENT); }
DI void await(u64* flg, int word0, int n, u64 e, int lane) {
  u64* p0 = flg + word0 + (lane < n ? lane : 0); u64* p1 = flg + word0 + (lane + 64 < n ? lane + 64 : 0);
  const unsigned long long t0 = __builtin_amdgcn_s_memrealtime();
  for (;;) {
    const u64 v0 = __hip_atomic_load(p0, __ATOMIC_RELAXED, __HIP_MEMORY_SCOPE_AGENT), v1 = __hip_atomic_load(p1, __ATOMIC_RELAXED, __HIP_MEMORY_SCOPE_AGENT);
    if (__all(v0 == e && v1 == e)) break;
    if (__builtin_amdgcn_s_memrealtime() - t0 > 2000000ull) { if (lane == 0) __hip_atomic_store(flg + F_TMO, e, __ATOMIC_RELAXED, __HIP_MEMORY_SCOPE_AGENT); break; }
    __builtin_amdgcn_s_sleep(1);
  }
}
constexpr __host__ __device__ bool in_(int s, int a, int n) { return s >= a && s < a + n; }
constexpr __host__ __device__ int npieces(int s) { return (s < 16 ? 10 : 0) + ((in_(s, S_T2, 16) || in_(s, S_T4, 32)) ? 8 : 0) + (in_(s, S_A, 32) ? 2 : 0) + (in_(s, S_T5, 16) ? 6 : 0); }

template <int IMM, bool SC1> DI void piece(unsigned sb, unsigned voff, rsrc_t rs, unsigned soff) {
  if constexpr (SC1) asm volatile("s_add_u32 m0, %0, %1\n\ts_nop 0\n\tbuffer_load_dwordx4 %2, %3, %4 offen sc1 lds" ::"s"(sb), "n"(IMM), "v"(voff), "s"(rs), "s"(soff) : "memory", "scc");
  else asm volatile("s_add_u32 m0, %0, %1\n\ts_nop 0\n\tbuffer_load_dwordx4 %2, %3, %4 offen lds" ::"s"(sb), "n"(IMM), "v"(voff), "s"(rs), "s"(soff) : "memory", "scc");
}
template <bool PS, bool QS> struct KKStream {
  rsrc_t rp, rq; unsigned vp[4], vq[4];
  DI void init(int lane, int w, const void* p, unsigned pbytes, unsigned prow0, const void* q, unsigned qbytes, unsigned qrow0, unsigned ld) {
    rp = make_rsrc(p, pbytes); rq = make_rsrc(q, qbytes);
#pragma unroll
    for (int g = 0; g < 4; ++g) { const unsigned kv = kc_voff(lane, g & 1, ld); vp[g] = kv + (prow0 + (4 * w + g) * 8) * ld; vq[g] = kv + (qrow0 + (4 * w + g) * 8) * ld; }
  }
  DI void issue(unsigned slot0, int w, int t) {
    const unsigned sb = slot0 + w * 4096, so = t * 128;
    piece<16384, QS>(sb, vq[0], rq, so); piece<17408, QS>(sb, vq[1], rq, so); piece<18432, QS>(sb, vq[2], rq, so); piece<19456, QS>(sb, vq[3], rq, so);
    piece<0, PS>(sb, vp[0], rp, so); piece<1024, PS>(sb, vp[1], rp, so); piece<2048, PS>(sb, vp[2], rp, so); piece<3072, PS>(sb, vp[3], rp, so);
  }
};
struct T1Stream {
  rsrc_t rw, rx; unsigned vw[6], vx[4];
  DI void init(const Args& a, int lane, int w, unsigned s0, unsigned ct) {
    rw = make_rsrc(a.wqkv, 1024 * 3072 * 2); rx = make_rsrc(a.Ax, 2048 * 1024 * 2);
#pragma unroll
    for (int p = 0; p < 3; ++p)
#pragma unroll
      for (int g = 0; g < 2; ++g) vw[2 * p + g] = ic_voff(lane, g, 6144) + (2 * w + g) * 8 * 6144u + p * 2048u + ct * 128u;
#pragma unroll
    for (int g = 0; g < 4; ++g) vx[g] = kc_voff(lane, g & 1, 2048) + (s0 + (4 * w + g) * 8) * 2048u;
  }
  DI void issue(unsigned slot0, int w, int t) {
    const unsigned sw = slot0 + w * 2048, sx = slot0 + 24576 + w * 4096, ow = t * (64u * 6144u), ox = t * 128;
    piece<0, false>(sw, vw[0], rw, ow); piece<1024, false>(sw, vw[1], rw, ow); piece<8192, false>(sw, vw[2], rw, ow);
    piece<9216, false>(sw, vw[3], rw, ow); piece<16384, false>(sw, vw[4], rw, ow); piece<17408, false>(sw, vw[5], rw, ow);
    piece<0, false>(sx, vx[0], rx, ox); piece<1024, false>(sx, vx[1], rx, ox); piece<2048, false>(sx, vx[2], rx, ox); piece<3072, false>(sx, vx[3], rx, ox);
  }
};
struct KVStream {
  rsrc_t rk, rv; unsigned vk, vv;
  DI void init(const Args& a, int lane, int w, int head) {
    rk = make_rsrc(a.Q + (size_t)(32 + head) * 65536, 65536 * 2 + 4096); rv = make_rsrc(a.Q + (size_t)(64 + head) * 65536, 65536 * 2);
    vk = k_voff(lane) + 32 * 64 + w * 1024; vv = v_voff(lane) + w * 8 * 4096;
  }
  DI void issue(unsigned slot0, int w, int t) { const unsigned sb = slot0 + w * 1024; piece<32768, true>(sb, vk, rk, t * 4096); piece<36864, true>(sb, vv, rv, t * 128); }
  DI void issue_first(unsigned slot0, int w) { const unsigned sb = slot0 + w * 1024; piece<32768, true>(sb, vk - 32 * 64, rk, 0); piece<36864, true>(sb, vk - 32 * 64, rk, 4096); }
};
struct T5Stream {
  rsrc_t rw, ro; unsigned vw[4], vo[2];
  DI void init(const Args& a, int lane, int w, unsigned n0, unsigned s5) {
    rw = make_rsrc(a.wo, 1024 * 1024 * 2); ro = make_rsrc(a.O, 2048 * 1024 * 2);
#pragma unroll
    for (int p = 0; p < 2; ++p)
#pragma unroll
      for (int g = 0; g < 2; ++g) vw[2 * p + g] = ic_voff(lane, g, 2048) + (2 * w + g) * 8 * 2048u + (n0 + p * 64) * 2u;
#pragma unroll
    for (int g = 0; g < 2; ++g) vo[g] = kc_voff(lane, g, 2048) + (s5 + (2 * w + g) * 8) * 2048u;
  }
  DI void issue(unsigned slot0, int w, int t) {
    const unsigned sb = slot0 + w * 2048, ow = t * (64u * 2048u), oo = t * 128;
    piece<0, false>(sb, vw[0], rw, ow); piece<1024, false>(sb, vw[1], rw, ow); piece<8192, false>(sb, vw[2], rw, ow); piece<9216, false>(sb, vw[3], rw, ow);
    piece<16384, true>(sb, vo[0], ro, oo); piece<17408, true>(sb, vo[1], ro, oo);
  }
};
struct Warm {
  rsrc_t rs; unsigned v, dv; unsigned long long mask;
  DI void fire() { l2warm(v, rs, PF_ON ? 0u : OOB); v += dv; }
  DI void step(int s) {
    const bool act = PF_ON && s < 64 && ((mask >> s) & 1);
    l2warm(v, rs, act ? 0u : OOB);
    if (act) v += dv;
  }
};
constexpr unsigned long long bit_(int s) { return 1ull << s; }
DI unsigned wl_x(int lane, int w, unsigned s0, int cl) { return (s0 + 32 * cl + (lane & 31)) * 2048u + ((lane >> 5) + 2 * w) * 128u; }
DI void warm_t1(Warm& f, const Args& a, int lane, int w, int rl, int cl, unsigned s0, unsigned ct) {
  const int wl = (w & 1) * 64 + lane;
  f.rs = w < 2 ? make_rsrc(a.Ax, 2048 * 1024 * 2) : make_rsrc(a.wqkv, 1024 * 3072 * 2);
  f.v = w < 2 ? wl_x(lane, w, s0, cl) : (wl < 96 ? (32 * rl + wl / 3) * 6144u + (wl % 3) * 2048u + ct * 128u : OOB);
  f.dv = w < 2 ? 512u : (wl < 96 ? 256u * 6144u : 0u);
}
constexpr __host__ __device__ int nwait(int s) { return npieces(s + 1) + npieces(s + 2) + (s == S_A - 1 ? 2 : 3); }
constexpr __host__ __device__ bool steady(int s0, int s1) { for (int s = s0; s < s1; ++s) if (nwait(s) != nwait(s0)) return false; return true; }
template <int S0, int S1, class F> DI void for_steps(F&& f) {
  if constexpr (S0 < S1) {
    if constexpr (steady(S0, S1) && S1 - S0 > 1) { for (int s = S0; s < S1; ++s) { wait_vm<nwait(S0)>(); f(s); } }
    else { wait_vm<nwait(S0)>(); f(S0); for_steps<S0 + 1, S1>(f); }
  }
}
DI void role_dma(const Args& a, const Ids& id) {
  TL_DECL;
  const int lane = id.lane, w = id.wid - 8, xcd = id.xcd, idx = id.idx;
  const int rl = idx & 7, cl = idx >> 3, head = xcd * 4 + cl, half = xcd & 1, tb = idx & 3, eb = idx >> 2;
  const unsigned s0 = ((xcd & 1) * 8 + rl) * 128, ct = (xcd >> 1) * 4 + cl, eg = ct * 128;
  const unsigned t0 = (xcd >> 1) * 512 + tb * 128, e0 = eb * 128;
  const unsigned n0 = ((xcd & 1) * 4 + tb) * 128, s5 = ((xcd >> 1) * 8 + eb) * 64;
  const int ph1 = (rl + cl) & 3, ph4 = (tb + eb) & 3;
  auto slot_of = [&](int s) { return id.lds0 + (s & 3) * SLOT; };
  Warm f;
  T1Stream t1; t1.init(a, lane, w, s0, ct);
  warm_t1(f, a, lane, w, rl, cl, s0, ct);
  TLB(0);
  t1.issue(slot_of(0), w, 0);
  f.fire();
  wait_vm<1>();
  barrier();
  TLB(1);
  t1.issue(slot_of(1), w, 1); t1.issue(slot_of(2), w, 2); t1.issue(slot_of(3), w, 3);
  f.fire();
  f.mask = (bit_(0) | bit_(4)) << ph1; f.step(0);
  l2warm(OOB, f.rs, OOB);
  for_steps<1, 8>([&](int s) { barrier(); TLB(s + 1); t1.issue(slot_of(s + 3), w, s + 3); f.step(s); });
  const void* wd = eg >= 1024 ? a.Wi : a.Wr;
  f.rs = w < 2 ? make_rsrc(a.Bx, 2048 * 1024 * 2) : make_rsrc(wd, 1024 * 1024 * 2);
  f.v = w < 2 ? wl_x(lane, w, s0, cl) : (w == 2 ? ((eg & 1023) + 16 * rl + (lane & 15)) * 2048u + (lane >> 4) * 128u : OOB);
  f.dv = w < 3 ? 512u : 0u; f.mask = T2PF == 0 ? (bit_(8) | bit_(10) | bit_(12) | bit_(14)) << (ph1 & 1)
         : (T2PF == 1 ? (bit_(8) | bit_(12)) << ph1 | (bit_(20) | bit_(22)) << (ph1 & 1) : (bit_(8) | bit_(11)) << (ph1 & 1) | bit_(14) << (ph1 & 1) | bit_(21) << (ph1 & 1));
  for_steps<8, 13>([&](int s) { barrier(); TLB(s + 1); t1.issue(slot_of(s + 3), w, s + 3); f.step(s); });
  KKStream<false, false> g2; g2.init(lane, w, a.Bx, 2048 * 1024 * 2, s0, wd, 1024 * 1024 * 2, eg & 1023, 2048);
  for_steps<13, S_A - 3>([&](int s) { barrier(); TLB(s + 1); g2.issue(slot_of(s + 3), w, s + 3 - S_T2); f.step(s); });
  KVStream kv; kv.init(a, lane, w, head);
  if (w == 0) { f.rs = make_rsrc(half ? a.Ci : a.Cr, 2048 * 2048 * 2); f.v = (t0 + 16 * eb + (lane & 15)) * 4096u + (lane >> 4) * 128u; f.dv = 512;
    f.mask = (bit_(27) | bit_(29) | bit_(S_T4 - 3) | bit_(S_T4 + 1) | bit_(S_T4 + 4) | bit_(S_T4 + 7) | bit_(S_T4 + 10) | bit_(S_T4 + 13)) << (ph4 & 1); }
  else if (w < 3) { f.rs = make_rsrc(a.ABt + (size_t)half * 1024 * 2048, 1024 * 2048 * 2);
    f.v = (e0 + 32 * tb + (lane & 31)) * 4096u + ((lane >> 5) + 2 * (w - 1)) * 128u + 512; f.dv = 512;
    f.mask = bit_(S_T4 - 3) | ((bit_(S_T4 - 1) | bit_(S_T4 + 2) | bit_(S_T4 + 5) | bit_(S_T4 + 8) | bit_(S_T4 + 11) | bit_(S_T4 + 14)) << (ph4 & 1)); }
  else { f.rs = make_rsrc(a.Q + (size_t)(32 + head) * 65536, 33 * 65536 * 2);
    f.v = lane < 16 ? 2048u + (rl * 16 + lane) * 128u + 16384u : (lane < 32 ? 32u * 65536 * 2 + (rl * 4 + ((lane >> 2) & 3)) * 4096u + (lane & 3) * 128u + 512u : OOB);
    f.dv = lane < 16 ? 16384u : (lane < 32 ? 512u : 0u);
    f.mask = bit_(S_A - 3) | ((bit_(S_A - 1) | bit_(S_A + 2) | bit_(36) | bit_(39) | bit_(43) | bit_(47)) << (idx & 1)); }
  for_steps<S_A - 3, S_A - 2>([&](int s) { barrier(); TLB(s + 1); kv.issue_first(slot_of(S_A - 1), w); g2.issue(slot_of(s + 3), w, s + 3 - S_T2); kv.issue(slot_of(s + 3), w, s + 3 - S_A); f.step(s); });
  for_steps<S_A - 2, 29>([&](int s) { barrier(); TLB(s + 1); g2.issue(slot_of(s + 3), w, s + 3 - S_T2); kv.issue(slot_of(s + 3), w, s + 3 - S_A); f.step(s); });
  for_steps<29, S_T4 - 3>([&](int s) { barrier(); TLB(s + 1); kv.issue(slot_of(s + 3), w, s + 3 - S_A); f.step(s); });
  KKStream<true, false> g4; g4.init(lane, w, a.ABt + (size_t)half * 1024 * 2048, 1024 * 2048 * 2, e0, half ? a.Ci : a.Cr, 2048 * 2048 * 2, t0, 4096);
  for_steps<S_T4 - 3, S_T4 - 2>([&](int s) { barrier(); TLB(s + 1); g4.issue(slot_of(s + 3), w, s + 3 - S_T4); kv.issue(slot_of(s + 3), w, s + 3 - S_A); f.step(s); });
  for_steps<S_T4 - 2, S_A + 29>([&](int s) { barrier(); TLB(s + 1); g4.issue(slot_of(s + 3), w, s + 3 - S_T4); kv.issue(slot_of(s + 3), w, s + 3 - S_A); f.step(s); });
  for_steps<S_A + 29, S_WO>([&](int s) { barrier(); TLB(s + 1); g4.issue(slot_of(s + 3), w, s + 3 - S_T4); f.step(s); });
  f.rs = make_rsrc(a.wo, 1024 * 1024 * 2); f.v = (128 * eb + (w & 1) * 64 + lane) * 2048u + n0 * 2 + (w >> 1) * 128u; f.dv = 0; f.mask = bit_(S_WO);
  for_steps<S_WO, S_T5 - 3>([&](int s) { barrier(); TLB(s + 1); g4.issue(slot_of(s + 3), w, s + 3 - S_T4); f.step(s); });
  T5Stream t5; t5.init(a, lane, w, n0, s5);
  for_steps<S_T5 - 3, S_T5 - 2>([&](int s) { barrier(); TLB(s + 1); t5.issue(slot_of(s + 3), w, s + 3 - S_T5); f.step(s); });
  for_steps<S_T5 - 2, S_END - 3>([&](int s) { barrier(); TLB(s + 1); t5.issue(slot_of(s + 3), w, s + 3 - S_T5); f.step(s); });
  for_steps<S_END - 3, S_END>([&](int s) { barrier(); TLB(s + 1); f.step(s); });
  TL_DUMP(8, 0);
}

DI void role_g0(const Args& a, const Ids& id) {
  TL_DECL; TLW(0, 0);
  const int wid = id.wid, wi = wid & 1, wj = wid >> 1;
  u64 ep = load_epoch(a);
  {
    const int lane = id.lane, rl = id.idx & 7, cl = id.idx >> 3, rb = (id.xcd & 1) * 8 + rl, ct = (id.xcd >> 1) * 4 + cl;
    const int s0 = rb * 128 + 64 * wj, head = 2 * ct + wi;
    Cons<32, 3, 2, true, 2> c;
#pragma unroll
    for (int i = 0; i < 3; ++i) c.pa[i] = id.lds0 + ic_raddr<32>(lane, 2 * i + wi);
#pragma unroll
    for (int k = 0; k < 4; ++k) c.qa[k] = id.lds0 + 24576 + kc_raddr<32>(lane, 64 * wj, k);
    c.zero();
    PlainHook hk;
    c.run<16, 4, SLOT>(hk, 0);
    barrier();
    ep = __builtin_amdgcn_readfirstlane((unsigned)ep) | ((u64)__builtin_amdgcn_readfirstlane((unsigned)(ep >> 32)) << 32);
    const int ln = fresh_lane();
    const unsigned stg = id.lds0 + wj * SLOT + 32768 + wi * 4096;
    const rsrc_t rq = make_rsrc(a.Q, 3u * 32 * 65536 * 2);
    const unsigned oq = (unsigned)(head * 65536 + s0 * 32) * 2u;
    u32x4 r[4];
    prep_tile64<true>(c.acc[0][0], QSCALE, stg, ln, r); prep_tile64<true>(c.acc[0][1], QSCALE, stg + 2048, ln, r + 2);
    barrier();
    commit64<true>(r, ln, rq, oq, 64); commit64<true>(r + 2, ln, rq, oq + 2048, 64);
    prep_tile64<false>(c.acc[1][0], 1.0f, stg, ln, r); prep_tile64<false>(c.acc[1][1], 1.0f, stg + 2048, ln, r + 2);
    barrier();
    commit64<true>(r, ln, rq, oq + 32 * 65536 * 2, 64); commit64<true>(r + 2, ln, rq, oq + 32 * 65536 * 2 + 2048, 64);
    {
      const int dd = ln & 31, h = ln >> 5;
#pragma unroll
      for (int jb = 0; jb < 2; ++jb)
#pragma unroll
        for (int g = 0; g < 2; ++g) { const f32x16& v = c.acc[2][jb];
          lds_wr128(stg + dd * 128 + (((4 * jb + 2 * g + h) ^ (dd & 7)) * 16), u32x4{cvtpk(v[8 * g], v[8 * g + 1]), cvtpk(v[8 * g + 2], v[8 * g + 3]), cvtpk(v[8 * g + 4], v[8 * g + 5]), cvtpk(v[8 * g + 6], v[8 * g + 7])}); }
      read128(stg, ln, r);
    }
    barrier();
    commit128<true>(r, ln, rq, (unsigned)((64 + head) * 65536 + s0) * 2u, 4096);
    idle(QKV_DRAIN);
    TLW(0, 2);
    signal(a.flg, F_QKV + ct * 64 + rb * 4 + wid, ep, ln);
    TLW(0, 3);
    idle(S_A - 3 - 20 - QKV_DRAIN);
    TLW(0, 4);
    if (wid == 0) await(a.flg, F_QKV + ((id.xcd * 4 + cl) >> 1) * 64, 64, ep, ln);
    TLW(0, 5);
    barrier();
  }
  {
    const int lane = fresh_lane(), r = lane & 31, h = lane >> 5;
    const int head = id.xcd * 4 + (id.idx >> 3), qb = id.idx & 7, q0 = qb * 256 + wid * 64;
    const rsrc_t rq = make_rsrc(a.Q + (size_t)head * 65536, 65536 * 2);
    AttnWave w;
#pragma unroll
    for (int b = 0; b < 2; ++b)
#pragma unroll
      for (int s = 0; s < 2; ++s) w.q[b][s] = __builtin_bit_cast(bf16x8, ld16s(rq, (q0 + 32 * b + r) * 64 + 32 * s + 16 * h, 0));
    idle(2);
    const unsigned kp = id.lds0 + ((S_A - 1) & 3) * SLOT + 32768;
    bf16x8 k0[4][2];
#pragma unroll
    for (int j = 0; j < 4; ++j) { k0[j][0] = lds_rd128(kp + k_raddr(lane, 0) + j * 2048); k0[j][1] = lds_rd128(kp + k_raddr(lane, 1) + j * 2048); }
#pragma unroll
    for (int b = 0; b < 2; ++b) {
      float mx = -INFINITY;
#pragma unroll
      for (int j = 3; j >= 0; --j) {
        f32x16 s = mfma32(k0[j][0], w.q[b][0], f32x16{}); s = mfma32(k0[j][1], w.q[b][1], s);
#pragma unroll
        for (int i = 0; i < 16; ++i) mx = __builtin_fmaxf(mx, s[i]);
        if (j == 0 && b == 0) w.S[0] = s;
      }
      const float nm = -(swap32_max(mx) + MARGIN);
#pragma unroll
      for (int i = 0; i < 16; ++i) w.NM[b][i] = nm;
      if (b == 0) w.S[0] = w.S[0] + nm;
      w.O[b] = f32x16{}; w.L[b] = f32x4{};
    }
    w.kf[0] = k0[0][0]; w.kf[1] = k0[0][1];
    w.S[1] = f32x16{};
    w.P[1][0] = u32x4{}; w.P[1][1] = u32x4{}; w.P[0][0] = u32x4{}; w.P[0][1] = u32x4{};
    w.vf[0] = bf16x8{}; w.vf[1] = bf16x8{};
    { const int m = lane & 15; const __bf16 one = (__bf16)1.0f, z = (__bf16)0.0f; const bool lo = (m == 0 || m == 4), hi = (m == 8 || m == 12);
      w.asel = bf16x8{lo ? one : z, lo ? one : z, lo ? one : z, lo ? one : z, hi ? one : z, hi ? one : z, hi ? one : z, hi ? one : z};
      w.sidx = (m == 4 || m == 12) ? 0xEEEE : 0x4444; }
    constexpr unsigned so = (S_A & 3) * SLOT;
    w.ka0 = id.lds0 + so + 32768 + k_raddr(lane, 0); w.ka1 = id.lds0 + so + 32768 + k_raddr(lane, 1);
    w.va0 = id.lds0 + so + 36864 + v_raddr(lane, 0); w.va1 = id.lds0 + so + 36864 + v_raddr(lane, 1);
    w.run<4, SLOT>(S_A & 3);
    barrier();
    TLW(0, 1);
    const int ln = fresh_lane(), r2 = ln & 31, h2 = ln >> 5;
    const unsigned stg = id.lds0 + ((S_A + 32 + wj) & 3) * SLOT + 32768 + wi * 4096;
    u32x4 ro[2];
#pragma unroll
    for (int b = 0; b < 2; ++b) {
      const int src = (r2 & 15) + (r2 >> 4) * 32;
      float l = __shfl(w.L[b][0], src) + __shfl(w.L[b][0], src + 16);
      float amax = 0.f;
#pragma unroll
      for (int i = 0; i < 16; ++i) amax = __builtin_fmaxf(amax, __builtin_fabsf(w.O[b][i]));
      bool nan = false;
#pragma unroll
      for (int i = 0; i < 16; ++i) nan |= !(w.O[b][i] == w.O[b][i]);
      const bool bad = !(l < 0x1p126f) || !(amax < 0x1p126f) || nan || (amax < 0x1p-115f) || a.force_slow;
      if (__any(bad)) attn_slow(make_rsrc(a.Q + (size_t)(32 + head) * 65536, 65536 * 2), make_rsrc(a.Q + (size_t)(64 + head) * 65536, 65536 * 2), w.q[b], ln, w.O[b], l);
      const float inv = 1.0f / l;
      const f32x16 v = w.O[b] * inv;
      lds_wr128(stg + b * 2048 + r2 * 64 + ((h2 ^ ((r2 >> 1) & 3)) * 16), u32x4{cvtpk(v[0], v[1]), cvtpk(v[2], v[3]), cvtpk(v[4], v[5]), cvtpk(v[6], v[7])});
      lds_wr128(stg + b * 2048 + r2 * 64 + (((2 + h2) ^ ((r2 >> 1) & 3)) * 16), u32x4{cvtpk(v[8], v[9]), cvtpk(v[10], v[11]), cvtpk(v[12], v[13]), cvtpk(v[14], v[15])});
#pragma unroll
      for (int m = 0; m < 2; ++m) { const int row = 32 * b + 16 * m + (ln >> 2), c = ln & 3; ro[m] = lds_rdu(stg + row * 64 + ((c ^ ((row >> 1) & 3)) * 16)); }
      barrier();
      commit64<true>(ro, ln, make_rsrc(a.O, 2048 * 1024 * 2), (unsigned)((q0 + 32 * b) * 1024 + head * 32) * 2u, 2048);
    }
    idle(O_DRAIN);
    TLW(0, 6);
    signal(a.flg, F_O + qb * 128 + head * 4 + wid, ep, ln);
    TLW(0, 7);
    idle(S_T5 - 3 - (S_A + 35 + O_DRAIN));
    TLW(0, 8);
    if (wid == 0) {
      await(a.flg, F_O + ((((id.xcd >> 1) * 8 + (id.idx >> 2)) * 64) >> 8) * 128, 128, ep, ln);
      if (blockIdx.x == 0 && ln == 0) __hip_atomic_store(a.flg + F_EPOCH, ep, __ATOMIC_RELAXED, __HIP_MEMORY_SCOPE_AGENT);
    }
    TLW(0, 9);
    idle(3);
  }
  {
    const int lane = fresh_lane(), nl = id.idx & 3, sl = id.idx >> 2;
    const int n0 = ((id.xcd & 1) * 4 + nl) * 128, s5 = ((id.xcd >> 1) * 8 + sl) * 64;
    Cons<32, 2, 1, true> c;
    constexpr unsigned so = (S_T5 & 3) * SLOT;
#pragma unroll
    for (int i = 0; i < 2; ++i) c.pa[i] = id.lds0 + so + ic_raddr<32>(lane, 2 * wi + i);
#pragma unroll
    for (int k = 0; k < 4; ++k) c.qa[k] = id.lds0 + so + 16384 + kc_raddr<32>(lane, 32 * wj, k);
    c.zero();
    PlainHook hk;
    c.run<16, 4, SLOT>(hk, S_T5 & 3);
    const int ln = fresh_lane();
    store_rows128<false, 32>(c.acc, 0, 0u, id.lds0 + (2 + wj) * SLOT + 32768 + wi * 4096, ln, make_rsrc(a.out, 2048 * 2048 * 2), (unsigned)((s5 + 32 * wj) * 2048 + n0 + 64 * wi) * 2u, 4096);
    TLW(0, 10); TL_DUMP(0, 1);
  }
}

DI void role_g1(const Args& a, const Ids& id) {
  TL_DECL;
  const int wi = id.wid & 1, wj = (id.wid >> 1) & 1, xcd = id.xcd, idx = id.idx;
  u64 ep = load_epoch(a);
  {
    const int lane = id.lane, rb = (xcd & 1) * 8 + (idx & 7), ct = (xcd >> 1) * 4 + (idx >> 3);
    const int s0 = rb * 128, eg = ct * 128;
    constexpr int SH = T2_SH, NB = 64 / SH;
    Cons<SH, NB, NB, false> c;
#pragma unroll
    for (int k = 0; k < c.NKS; ++k) { c.pa[k] = id.lds0 + kc_raddr<SH>(lane, 64 * wi, k); c.qa[k] = id.lds0 + 16384 + kc_raddr<SH>(lane, 64 * wj, k); }
    c.zero();
    idle(8);
    { unsigned lo = __builtin_amdgcn_readfirstlane((unsigned)ep), hi = __builtin_amdgcn_readfirstlane((unsigned)(ep >> 32)); asm volatile("" : "+s"(lo), "+s"(hi)); ep = lo | ((u64)hi << 32); }
    idle(8);
    PlainHook hk;
    c.template run<16, 4, SLOT>(hk, 0);
    barrier();
    const int ln = fresh_lane();
    const unsigned stg = id.lds0 + (id.wid & 3) * 8192;
    const unsigned sgn = eg >= 1024 ? 0x80008000u : 0u;
    const rsrc_t rab = make_rsrc(a.ABt, 2048u * 2048 * 2);
    u32x4 r[4];
    prep_rows128<SH>(c.acc, 0, sgn, stg, ln, r);
    barrier();
    commit128<true>(r, ln, rab, (unsigned)((eg + 64 * wj) * 2048 + s0 + 64 * wi) * 2u, 4096);
    prep_rows128<SH>(c.acc, 1, sgn, stg, ln, r);
    barrier();
    commit128<true>(r, ln, rab, (unsigned)((eg + 64 * wj + 32) * 2048 + s0 + 64 * wi) * 2u, 4096);
    idle(AB_DRAIN);
    signal(a.flg, F_AB + ct * 64 + rb * 4 + (id.wid & 3), ep, ln);
    idle(S_T4 - 3 - 35 - AB_DRAIN);
    if (id.wid == 4) await(a.flg, F_AB + ((xcd & 1) * 8 + (idx >> 2)) * 64, 64, ep, ln);
    idle(3);
  }
  {
    const int lane = fresh_lane(), half = xcd & 1, t0 = (xcd >> 1) * 512 + (idx & 3) * 128, e0 = (idx >> 2) * 128;
    constexpr int SH = G_SH, NB = 64 / SH;
    Cons<SH, NB, NB, false> c;
    constexpr unsigned so = (S_T4 & 3) * SLOT;
#pragma unroll
    for (int k = 0; k < c.NKS; ++k) { c.pa[k] = id.lds0 + so + kc_raddr<SH>(lane, 64 * wi, k); c.qa[k] = id.lds0 + so + 16384 + kc_raddr<SH>(lane, 64 * wj, k); }
    c.zero();
    PlainHook hk;
    c.template run<32, 4, SLOT>(hk, S_T4 & 3);
    barrier();
    TLW(4, 1); TL_DUMP(4, 2);
    const int ln = fresh_lane();
    auto get = [&](int m, int e) -> float { if constexpr (SH == 16) return c.acc[(2 * m + (e >> 2)) / NB][(2 * m + (e >> 2)) % NB][e & 3]; else return c.acc[(m >> 1) / NB][(m >> 1) % NB][(m & 1) * 8 + e]; };
    auto add = [&](int m, int e, float x) { if constexpr (SH == 16) c.acc[(2 * m + (e >> 2)) / NB][(2 * m + (e >> 2)) % NB][e & 3] += x; else c.acc[(m >> 1) / NB][(m >> 1) % NB][(m & 1) * 8 + e] += x; };
    if (wj != half) {
      const rsrc_t rx = make_rsrc(a.X + (size_t)(blockIdx.x * 2 + wi) * 2048, 8192);
#pragma unroll
      for (int m = 0; m < 8; ++m)
        st16s(rx, ln * 16, m * 1024, u32x4{cvtpk(get(m, 0), get(m, 1)), cvtpk(get(m, 2), get(m, 3)), cvtpk(get(m, 4), get(m, 5)), cvtpk(get(m, 6), get(m, 7))});
      idle(3);
      signal(a.flg, F_P + (blockIdx.x ^ 1) * 2 + wi, ep, ln);
      return;
    }
    idle(9);
    await(a.flg, F_P + blockIdx.x * 2 + wi, 1, ep, ln);
    { const rsrc_t rx = make_rsrc(a.X + (size_t)((blockIdx.x ^ 1) * 2 + wi) * 2048, 8192);
      u32x4 y[8];
#pragma unroll
      for (int m = 0; m < 8; ++m) y[m] = ld16s(rx, ln * 16, m * 1024);
      idle(6);
#pragma unroll
      for (int m = 0; m < 8; ++m)
#pragma unroll
        for (int e = 0; e < 4; ++e) { add(m, 2 * e, __uint_as_float(y[m][e] << 16)); add(m, 2 * e + 1, __uint_as_float(y[m][e] & 0xffff0000u)); } }
    const unsigned stg = id.lds0 + 32768 + wi * 4096;
    const rsrc_t ro = make_rsrc(a.out, 2048 * 2048 * 2);
#pragma unroll
    for (int p = 0; p < 2; ++p)
      store_rows128<false, SH>(c.acc, p, 0u, stg, ln, ro, (unsigned)((t0 + 64 * wj + 32 * p) * 2048 + 1024 + e0 + 64 * wi) * 2u, 4096);
  }
}

DI Ids make_ids(char* lds) { Ids id; id.lane = threadIdx.x & 63; id.wid = __builtin_amdgcn_readfirstlane(threadIdx.x >> 6); id.xcd = blockIdx.x & 7; id.idx = blockIdx.x >> 3; id.lds0 = (unsigned)(size_t)(LAS char*)lds; return id; }
__global__ __launch_bounds__(768, 3) void mega_kernel(Args a) {
  extern __shared__ __attribute__((aligned(16))) char lds[];
  const Ids id = make_ids(lds);
  if (id.wid < 4) role_g0(a, id);
  else if (id.wid < 8) role_g1(a, id);
  else role_dma(a, id);
}

extern "C" void kernel_launch(void* const* d_in, const int* in_sizes, int n_in, void* d_out, int out_size, void* d_ws, size_t ws_size, hipStream_t stream) {
  u16* ws = (u16*)d_ws; const size_t M = (size_t)2048 * 1024;
  Args a;
  a.Ax = (const u16*)d_in[0]; a.wqkv = (const u16*)d_in[1]; a.wo = (const u16*)d_in[2]; a.Bx = (const u16*)d_in[3];
  a.Cr = (const u16*)d_in[4]; a.Ci = (const u16*)d_in[5]; a.Wr = (const u16*)d_in[6]; a.Wi = (const u16*)d_in[7];
  a.Q = ws; a.ABt = ws + 3 * M; a.O = ws + 5 * M; a.X = (float*)(ws + 6 * M); a.out = (u16*)d_out;
  a.flg = (unsigned long long*)(ws + 12 * M); a.dbg = nullptr; a.force_slow = 0; a.mode = 0;
  (void)hipFuncSetAttribute((const void*)mega_kernel, hipFuncAttributeMaxDynamicSharedMemorySize, LDS_ALL);
  mega_kernel<<<256, 768, LDS_ALL, stream>>>(a);
}
```

```cpp
#include <hip/hip_runtime.h>
#include <type_traits>

typedef unsigned short u16;
typedef unsigned long long u64;
typedef __bf16 bf16x8 __attribute__((ext_vector_type(8)));
typedef __bf16 bf16x16 __attribute__((ext_vector_type(16)));
typedef __bf16 bf16x2 __attribute__((ext_vector_type(2)));
typedef short s16x4 __attribute__((ext_vector_type(4)));
typedef short s16x8 __attribute__((ext_vector_type(8)));
typedef float f32x2 __attribute__((ext_vector_type(2)));
typedef float f32x4 __attribute__((ext_vector_type(4)));
typedef float f32x16 __attribute__((ext_vector_type(16)));
typedef unsigned u32x4 __attribute__((ext_vector_type(4)));
typedef unsigned u32x2 __attribute__((ext_vector_type(2)));
typedef unsigned u32x8 __attribute__((ext_vector_type(8)));
typedef __amdgpu_buffer_rsrc_t rsrc_t;
#define LAS __attribute__((address_space(3)))
#define DI __device__ __forceinline__
#define SBAR() __builtin_amdgcn_sched_barrier(0)
#define PIN(x) asm volatile("" : "+v"(x))

constexpr int SLOT = 40960, LDS_ALL = 4 * SLOT;
constexpr int S_T2 = 19;
constexpr int S_A = 27;
constexpr int S_T4 = S_T2 + 25;
constexpr int S_T5 = S_T4 + 32;
constexpr int S_END = S_T5 + 16;
constexpr int S_WO1 = S_T4 + 3, S_WO2 = S_T4 + 25;
static_assert(S_T2 >= 17 && S_A - 3 >= 22 && S_A - 2 <= S_T2 + 13 && S_T4 - 3 >= S_T2 + 21 && S_T4 - 2 <= S_A + 29 && S_T5 - 3 >= S_A + 36 &&
              S_WO1 < S_A + 29 && S_WO2 >= S_A + 29 && S_WO2 < S_T5 - 3 && S_T4 + 15 < 64, "schedule");
constexpr float QSCALE = 0.17677669529663687f * 1.4426950408889634f;
constexpr float MARGIN = 100.0f;

struct Args {
  const u16 *Ax, *wqkv, *wo, *Bx, *Cr, *Ci, *Wr, *Wi;
  u16 *Q;
  u16 *ABt;
  u16 *O;
  u16 *X;
  u16 *out;
  u64 *flg;
};
constexpr int F_QKV = 0, F_AB = 1024, F_O = 2048, F_P = 3072, F_EPOCH = 3600, F_TMO = 3632;
struct Ids { int lane, wid, xcd, idx; unsigned lds0; };

DI int fresh_lane() { int l; asm volatile("v_mbcnt_lo_u32_b32 %0, -1, 0\n\tv_mbcnt_hi_u32_b32 %0, -1, %0" : "=v"(l)); return l; }
DI rsrc_t make_rsrc(const void* p, unsigned bytes) { return __builtin_amdgcn_make_buffer_rsrc((void*)p, 0, bytes, 0x00020000); }
DI u32x4 ld16s(rsrc_t rs, unsigned voff, unsigned soff) { return __builtin_amdgcn_raw_buffer_load_b128(rs, voff, soff, 16); }
template <bool SC1> DI void st16(rsrc_t rs, unsigned voff, unsigned soff, u32x4 v) { __builtin_amdgcn_raw_buffer_store_b128(v, rs, voff, soff, SC1 ? 16 : 0); }
template <int N> DI void wait_vm() { asm volatile("s_waitcnt vmcnt(%0)" ::"n"(N) : "memory"); }
DI void barrier() { asm volatile("s_barrier" ::: "memory"); }
DI void sync_lds() { asm volatile("s_waitcnt lgkmcnt(0)\n\ts_barrier" ::: "memory"); }
DI void idle(int n) {
#pragma nounroll
  for (int i = 0; i < n; ++i) barrier();
}
DI unsigned cvtpk(float lo, float hi) { f32x2 v = {lo, hi}; bf16x2 b = __builtin_convertvector(v, bf16x2); return __builtin_bit_cast(unsigned, b); }
DI f32x16 mfma32(bf16x8 a, bf16x8 b, f32x16 c) { return __builtin_amdgcn_mfma_f32_32x32x16_bf16(a, b, c, 0, 0, 0); }
DI f32x4 mfma16(bf16x8 a, bf16x8 b, f32x4 c) { return __builtin_amdgcn_mfma_f32_16x16x32_bf16(a, b, c, 0, 0, 0); }
DI bf16x8 lds_rd128(unsigned addr) { return *(const LAS bf16x8*)(size_t)addr; }
DI u32x4 lds_rdu(unsigned addr) { return *(const LAS u32x4*)(size_t)addr; }
DI void lds_wr64(unsigned addr, u32x2 v) { *(LAS u32x2*)(size_t)addr = v; }
DI void lds_wr128(unsigned addr, u32x4 v) { *(LAS u32x4*)(size_t)addr = v; }
DI s16x4 lds_rdtr(unsigned addr) { return __builtin_amdgcn_ds_read_tr16_b64_v4i16((LAS s16x4*)(size_t)addr); }
DI bf16x8 cat8(s16x4 a, s16x4 b) { s16x8 r = __builtin_shufflevector(a, b, 0, 1, 2, 3, 4, 5, 6, 7); return __builtin_bit_cast(bf16x8, r); }
DI bf16x16 cat16(u32x4 a, u32x4 b) { u32x8 r = __builtin_shufflevector(a, b, 0, 1, 2, 3, 4, 5, 6, 7); return __builtin_bit_cast(bf16x16, r); }
DI bf16x8 as_bf(u32x4 v) { return __builtin_bit_cast(bf16x8, v); }
DI float ex2(float x) { return __builtin_amdgcn_exp2f(x); }

DI u64 load_epoch(const Args& a) { return __hip_atomic_load(a.flg + F_EPOCH, __ATOMIC_RELAXED, __HIP_MEMORY_SCOPE_AGENT) + 1; }
DI u64 uniform64(u64 v) { unsigned lo = __builtin_amdgcn_readfirstlane((unsigned)v), hi = __builtin_amdgcn_readfirstlane((unsigned)(v >> 32)); asm volatile("" : "+s"(lo), "+s"(hi)); return lo | ((u64)hi << 32); }
DI void signal(u64* flg, int word, u64 e, int lane) { wait_vm<0>(); if (lane == 0) __hip_atomic_store(flg + word, e, __ATOMIC_RELAXED, __HIP_MEMORY_SCOPE_AGENT); }
DI void await(u64* flg, int word0, int n, u64 e, int lane) {
  u64* p0 = flg + word0 + (lane < n ? lane : 0); u64* p1 = flg + word0 + (lane + 64 < n ? lane + 64 : 0);
  const u64 t0 = __builtin_amdgcn_s_memrealtime();
  for (;;) {
    const u64 v0 = __hip_atomic_load(p0, __ATOMIC_RELAXED, __HIP_MEMORY_SCOPE_AGENT), v1 = __hip_atomic_load(p1, __ATOMIC_RELAXED, __HIP_MEMORY_SCOPE_AGENT);
    if (__all(v0 == e && v1 == e)) break;
    if (__builtin_amdgcn_s_memrealtime() - t0 > 100000000ull) { if (lane == 0) __hip_atomic_store(flg + F_TMO, e, __ATOMIC_RELAXED, __HIP_MEMORY_SCOPE_AGENT); break; }
    __builtin_amdgcn_s_sleep(1);
  }
}

DI unsigned kc_voff(int lane, int par, unsigned ldb) { return (unsigned)(lane >> 3) * ldb + (unsigned)(((lane & 7) ^ (((lane >> 4) + 4 * par) & 7)) * 16); }
DI unsigned ic_voff(int lane, int par, unsigned ldb) { const int c = lane & 7, u = (c >> 1) ^ (((lane >> 4) & 1) << 1) ^ par; return (unsigned)(lane >> 3) * ldb + (unsigned)((u * 2 + (c & 1)) * 16); }
DI unsigned kc_raddr(int lane, int row0, int ks) { const int row = row0 + (lane & 15); return (unsigned)(row * 128 + (((4 * ks + (lane >> 4)) ^ ((row >> 1) & 7)) * 16)); }
DI unsigned ic_raddr(int lane, int cb) {
  const int g = lane >> 4, q = (lane & 15) >> 2, p = lane & 3, m = (((q >> 1) & 1) << 1) ^ (g & 1);
  return (unsigned)((cb >> 2) * 8192 + (8 * g + q) * 128 + (((cb & 3) ^ m) * 32) + p * 8);
}

template <int NI, int NJ, bool PIC, int SWAPI = 99, bool ROLLP = false> struct Cons {
  static constexpr int NPA = PIC ? NI : 2, NM = NI * NJ, PI = PIC ? 2 : 1, NIT = NJ + NI * PI;
  static constexpr int RG = NM - 1 < NIT ? NM - 1 : NIT;
  unsigned pa[NPA], qa[2];
  f32x4 acc[NI][NJ];
  bf16x8 p0[NI], q0[NJ], p1[NI], q1[NJ];
  s16x4 tlo;
  template <int KS, int K> DI void rd(bf16x8 (&p)[NI], bf16x8 (&q)[NJ]) {
    if constexpr (K == 0) q[0] = lds_rd128(qa[KS]);
    else if constexpr (K > PI && K < PI + NJ) q[K - PI] = lds_rd128(qa[KS] + (K - PI) * 2048);
    else { constexpr int r = K <= PI ? K - 1 : K - NJ, i = r / PI, hf = r % PI;
      if constexpr (!PIC) p[i] = lds_rd128(pa[KS] + i * 2048);
      else if constexpr (hf == 0) tlo = lds_rdtr(pa[i] + KS * 4096);
      else p[i] = cat8(tlo, lds_rdtr(pa[i] + KS * 4096 + 512)); }
  }
  static constexpr int free_gap(int R) {
    if (ROLLP) return R < NPA ? (R < NI - 1 ? (R + 1) * NJ : NM - 1) : (R == NPA ? 0 : 5);
    if (R == NPA) return PIC ? 0 : 1;
    if (!PIC && R == 0) return 0;
    const int g = RG + (R < NPA ? (PIC ? R + 1 : 1) : 0) / 2;
    return g < NM ? g : NM - 1;
  }
  template <int M, int R> DI void adv_at(int delta) {
    if constexpr (R < NPA + 2) {
      if constexpr (free_gap(R) == M) { if constexpr (R < NPA) { pa[R] += delta; PIN(pa[R]); } else { qa[R - NPA] += delta; PIN(qa[R - NPA]); } }
      adv_at<M, R + 1>(delta);
    }
  }
  template <int KS, bool RD, int M> DI void grp(bf16x8 (&cp)[NI], bf16x8 (&cq)[NJ], bf16x8 (&np)[NI], bf16x8 (&nq)[NJ], int delta) {
    if constexpr (M < NM) {
      constexpr int mi = M / NJ, mj = (mi & 1) ? NJ - 1 - M % NJ : M % NJ;
      if constexpr (mi >= SWAPI) acc[mi][mj] = mfma16(cq[mj], ROLLP ? p0[mi] : cp[mi], acc[mi][mj]);
      else acc[mi][mj] = mfma16(ROLLP ? p0[mi] : cp[mi], cq[mj], acc[mi][mj]);
      SBAR();
      if constexpr (RD && KS == 0 && M == (ROLLP ? 2 : 0)) { sync_lds(); SBAR(); }
      if constexpr (RD && ROLLP) {
        static_assert(!ROLLP || (NJ == 4 && PIC), "rolling form");
        constexpr int qj = KS == 0 ? (M == 2 ? 0 : (M == 5 ? 1 : (M == 6 ? 2 : (M == 9 ? 3 : -1)))) : (M < 3 ? M : (M == 5 ? 3 : -1));
        if constexpr (qj >= 0) nq[qj] = lds_rd128(qa[KS] + qj * 2048);
        if constexpr (M % NJ == NJ - 1) {
          if constexpr (mi < NI - 1) tlo = lds_rdtr(pa[mi] + KS * 4096); else p0[mi] = cat8(lds_rdtr(pa[mi] + KS * 4096), lds_rdtr(pa[mi] + KS * 4096 + 512)); }
        if constexpr (M % NJ == 0 && M > 0) p0[mi - 1] = cat8(tlo, lds_rdtr(pa[mi - 1] + KS * 4096 + 512));
      } else if constexpr (RD && M < RG) {
        constexpr int lo = (M * NIT + RG - 1) / RG, hi = M + 1 < RG ? ((M + 1) * NIT + RG - 1) / RG : NIT;
        static_assert(hi - lo <= 2, "reads per gap");
        if constexpr (lo < hi) rd<KS, lo>(np, nq);
        if constexpr (lo + 1 < hi) rd<KS, lo + 1>(np, nq);
      }
      if constexpr (RD && KS == 1) adv_at<M, 0>(delta);
      SBAR();
      grp<KS, RD, M + 1>(cp, cq, np, nq, delta);
    }
  }
  template <int NT> DI void run(int s0) {
#pragma unroll
    for (int i = 0; i < NI; ++i) {
#pragma unroll
      for (int j = 0; j < NJ; ++j) acc[i][j] = f32x4{};
      if constexpr (ROLLP) p0[i] = bf16x8{}; else p1[i] = bf16x8{};
    }
#pragma unroll
    for (int j = 0; j < NJ; ++j) q1[j] = bf16x8{};
    int slot = s0;
    grp<0, true, 0>(p1, q1, p0, q0, 0);
    for (int t = 1; t < NT; ++t) {
      const int d = slot == 3 ? -3 * SLOT : SLOT; slot = (slot + 1) & 3;
      grp<1, true, 0>(p0, q0, p1, q1, d);
      grp<0, true, 0>(p1, q1, p0, q0, 0);
    }
    grp<1, true, 0>(p0, q0, p1, q1, 0);
    grp<0, false, 0>(p1, q1, p0, q0, 0);
  }
};

template <bool SCALE> DI void prep64(const f32x4& t00, const f32x4& t10, const f32x4& t01, const f32x4& t11, float sc, unsigned stg, int lane, u32x4* r) {
  const int j = lane & 15, g = lane >> 4;
  auto put = [&](const f32x4& v, int ii, int jj) { const int row = 16 * jj + j;
    const u32x2 w = SCALE ? u32x2{cvtpk(v[0] * sc, v[1] * sc), cvtpk(v[2] * sc, v[3] * sc)} : u32x2{cvtpk(v[0], v[1]), cvtpk(v[2], v[3])};
    lds_wr64(stg + row * 64 + (((2 * ii + (g >> 1)) ^ ((row >> 1) & 3)) * 16) + (g & 1) * 8, w); };
  put(t00, 0, 0); put(t10, 1, 0); put(t01, 0, 1); put(t11, 1, 1);
#pragma unroll
  for (int m = 0; m < 2; ++m) { const int rr = 16 * m + (lane >> 2), c = lane & 3; r[m] = lds_rdu(stg + rr * 64 + ((c ^ ((rr >> 1) & 3)) * 16)); }
}
template <bool SC1> DI void commit64(const u32x4* r, int lane, rsrc_t rs, unsigned soff, unsigned ldb) {
#pragma unroll
  for (int m = 0; m < 2; ++m) st16<SC1>(rs, (16 * m + (lane >> 2)) * ldb + (lane & 3) * 16, soff, r[m]);
}
DI void read128(unsigned stg, int lane, u32x4* r) {
#pragma unroll
  for (int m = 0; m < 4; ++m) { const int row = 8 * m + (lane >> 3), c = lane & 7; r[m] = lds_rdu(stg + row * 128 + ((c ^ (row & 7)) * 16)); }
}
template <bool SC1> DI void commit128(const u32x4* r, int lane, rsrc_t rs, unsigned soff, unsigned ldb) {
#pragma unroll
  for (int m = 0; m < 4; ++m) st16<SC1>(rs, (8 * m + (lane >> 3)) * ldb + (lane & 7) * 16, soff, r[m]);
}
template <int NJ> DI void prep128(const f32x4 (&acc)[4][NJ], int j32, unsigned sgn, unsigned stg, int lane, u32x4* r) {
  const int j = lane & 15, g = lane >> 4;
#pragma unroll
  for (int jj = 0; jj < 2; ++jj)
#pragma unroll
    for (int ii = 0; ii < 4; ++ii) { const f32x4& v = acc[ii][2 * j32 + jj]; const int row = 16 * jj + j;
      lds_wr64(stg + row * 128 + (((2 * ii + (g >> 1)) ^ (row & 7)) * 16) + (g & 1) * 8, u32x2{cvtpk(v[0], v[1]) ^ sgn, cvtpk(v[2], v[3]) ^ sgn}); }
  read128(stg, lane, r);
}

DI int iperm(int r) { const int b = r & 3, h = (r >> 2) & 1, t = r >> 3; return b + 4 * (t & 1) + 8 * h + 16 * (t >> 1); }
DI unsigned k_voff(int lane) { return (unsigned)((lane >> 2) * 64 + (((lane & 3) ^ ((lane >> 4) & 3)) * 16)); }
DI unsigned v_voff(int lane) { const int c = lane & 7; return (unsigned)((lane >> 3) * 4096 + ((c & 4) | ((c & 3) ^ ((lane >> 4) & 3))) * 16); }
DI unsigned k_raddr(int lane, int s) { const int r = lane & 31, h = lane >> 5; return (unsigned)(r * 64 + (((2 * s + h) ^ ((r >> 2) & 3)) * 16)); }
DI unsigned v_raddr(int lane, int s) { const int d = iperm(lane & 31), h = lane >> 5; return (unsigned)(d * 128 + (((2 * s + h) ^ ((d >> 1) & 3)) * 16)); }
DI float swap32_max(float m) { auto rr = __builtin_amdgcn_permlane32_swap(__float_as_uint(m), __float_as_uint(m), false, false); return __builtin_fmaxf(__uint_as_float(rr[0]), __uint_as_float(rr[1])); }
DI float swap32_sum(float m) { auto rr = __builtin_amdgcn_permlane32_swap(__float_as_uint(m), __float_as_uint(m), false, false); return __uint_as_float(rr[0]) + __uint_as_float(rr[1]); }

struct AttnWave {
  f32x16 S[2], NM[2], O[2]; f32x4 L[2];
  u32x4 P[2][2];
  bf16x8 q[2][2], kf[2], vf[2];
  bf16x8 asel; int sidx;
  unsigned ka0, ka1, va0, va1;
  template <int X> DI void exp4(int i) { S[X][i] = ex2(S[X][i]); S[X][i + 1] = ex2(S[X][i + 1]); S[X][i + 2] = ex2(S[X][i + 2]); S[X][i + 3] = ex2(S[X][i + 3]); PIN(S[X]); }
  template <int X> DI void cvt2(int i) { P[X][i >> 3][(i >> 1) & 3] = cvtpk(S[X][i], S[X][i + 1]); P[X][i >> 3][((i >> 1) & 3) + 1] = cvtpk(S[X][i + 2], S[X][i + 3]); PIN(P[X][i >> 3]); }
  template <int X, int RD, bool AFTER_RD> DI void slot(int delta = 0) {
    constexpr int Y = X ^ 1, B = RD - 1;
    if constexpr (AFTER_RD) __builtin_amdgcn_s_waitcnt(0xC27F);
    S[Y] = mfma32(kf[0], q[Y][0], NM[Y]); SBAR();
    if constexpr (RD == 1) { ka0 += delta; ka1 += delta; va0 += delta; va1 += delta; PIN(ka0); PIN(ka1); PIN(va0); PIN(va1); sync_lds(); SBAR(); }
    exp4<X>(0); SBAR();
    S[Y] = mfma32(kf[1], q[Y][1], S[Y]); SBAR();
    if constexpr (RD) { kf[0] = lds_rd128(ka0 + B * 2048); SBAR(); }
    exp4<X>(4); cvt2<X>(0); SBAR();
    if constexpr (AFTER_RD) __builtin_amdgcn_s_waitcnt(0xC07F);
    O[Y] = mfma32(vf[0], as_bf(P[Y][0]), O[Y]); SBAR();
    if constexpr (RD) { kf[1] = lds_rd128(ka1 + B * 2048); SBAR(); }
    exp4<X>(8); cvt2<X>(4); SBAR();
    O[Y] = mfma32(vf[1], as_bf(P[Y][1]), O[Y]); SBAR();
    if constexpr (RD) { vf[0] = lds_rd128(va0 + B * 64); SBAR(); }
    exp4<X>(12); cvt2<X>(8); SBAR();
    L[Y] = __builtin_amdgcn_smfmac_f32_16x16x64_bf16(asel, cat16(P[Y][0], P[Y][1]), L[Y], sidx, 0, 0); SBAR();
    if constexpr (RD) { vf[1] = lds_rd128(va1 + B * 64); SBAR(); }
    cvt2<X>(12); SBAR();
  }
  DI void run(int sl) {
    slot<0, 1, false>(0); slot<1, 0, true>();
    for (int t = 1; t < 32; ++t) {
      const int d = sl == 3 ? -3 * SLOT : SLOT; sl = (sl + 1) & 3;
      slot<0, 2, false>(); slot<1, 0, true>(); slot<0, 1, false>(d); slot<1, 0, true>();
    }
    slot<0, 2, false>(); slot<1, 0, true>();
    O[1] = mfma32(vf[0], as_bf(P[1][0]), O[1]);
    O[1] = mfma32(vf[1], as_bf(P[1][1]), O[1]);
    L[1] = __builtin_amdgcn_smfmac_f32_16x16x64_bf16(asel, cat16(P[1][0], P[1][1]), L[1], sidx, 0, 0);
  }
};
DI void attn_exact(rsrc_t rk, rsrc_t rv, const bf16x8 (&q)[2], int lane, f32x16& O, float& l) {
  const int r = lane & 31, h = lane >> 5;
  float m = -INFINITY; l = 0.f; O = f32x16{};
  const unsigned ko = r * 64 + h * 16, vo = iperm(r) * 4096 + h * 16;
  for (int j = 0; j < 64; ++j) {
    f32x16 s = mfma32(as_bf(ld16s(rk, ko, j * 2048)), q[0], f32x16{});
    s = mfma32(as_bf(ld16s(rk, ko + 32, j * 2048)), q[1], s);
    float mx = s[0];
#pragma unroll
    for (int i = 1; i < 16; ++i) mx = __builtin_fmaxf(mx, s[i]);
    const float mn = __builtin_fmaxf(m, swap32_max(mx));
    const float f = ex2(m - mn); m = mn;
    float ps = 0.f; u32x4 p0, p1;
#pragma unroll
    for (int i = 0; i < 16; ++i) s[i] = ex2(s[i] - mn);
#pragma unroll
    for (int i = 0; i < 4; ++i) { p0[i] = cvtpk(s[2 * i], s[2 * i + 1]); p1[i] = cvtpk(s[8 + 2 * i], s[9 + 2 * i]); }
#pragma unroll
    for (int i = 0; i < 4; ++i) { ps += __uint_as_float(p0[i] << 16) + __uint_as_float(p0[i] & 0xffff0000u) + __uint_as_float(p1[i] << 16) + __uint_as_float(p1[i] & 0xffff0000u); }
    l = l * f + swap32_sum(ps);
    O = O * f;
    O = mfma32(as_bf(ld16s(rv, vo, j * 64)), as_bf(p0), O);
    O = mfma32(as_bf(ld16s(rv, vo + 32, j * 64)), as_bf(p1), O);
  }
}

template <int IMM, bool SC1> DI void piece(unsigned sb, unsigned voff, rsrc_t rs, unsigned soff) {
  if constexpr (SC1) asm volatile("s_add_u32 m0, %0, %1\n\ts_nop 0\n\tbuffer_load_dwordx4 %2, %3, %4 offen sc1 lds" ::"s"(sb), "n"(IMM), "v"(voff), "s"(rs), "s"(soff) : "memory", "scc");
  else asm volatile("s_add_u32 m0, %0, %1\n\ts_nop 0\n\tbuffer_load_dwordx4 %2, %3, %4 offen lds" ::"s"(sb), "n"(IMM), "v"(voff), "s"(rs), "s"(soff) : "memory", "scc");
}
template <bool PS> struct KKStream {
  rsrc_t rp, rq; unsigned vp[4], vq[4];
  DI void init(int lane, int w, const void* p, unsigned pbytes, unsigned prow0, const void* q, unsigned qbytes, unsigned qrow0, unsigned ld) {
    rp = make_rsrc(p, pbytes); rq = make_rsrc(q, qbytes);
#pragma unroll
    for (int g = 0; g < 4; ++g) { const unsigned kv = kc_voff(lane, g & 1, ld); vp[g] = kv + (prow0 + (4 * w + g) * 8) * ld; vq[g] = kv + (qrow0 + (4 * w + g) * 8) * ld; }
  }
  DI void issue(unsigned slot0, int w, int t) {
    const unsigned sb = slot0 + w * 4096, so = t * 128;
    piece<16384, false>(sb, vq[0], rq, so); piece<17408, false>(sb, vq[1], rq, so); piece<18432, false>(sb, vq[2], rq, so); piece<19456, false>(sb, vq[3], rq, so);
    piece<0, PS>(sb, vp[0], rp, so); piece<1024, PS>(sb, vp[1], rp, so); piece<2048, PS>(sb, vp[2], rp, so); piece<3072, PS>(sb, vp[3], rp, so);
  }
};
struct T1Stream {
  rsrc_t rw, rx; unsigned vw[6], vx[4];
  DI void init(const Args& a, int lane, int w, unsigned s0, unsigned ct) {
    rw = make_rsrc(a.wqkv, 1024 * 3072 * 2); rx = make_rsrc(a.Ax, 2048 * 1024 * 2);
#pragma unroll
    for (int p = 0; p < 3; ++p)
#pragma unroll
      for (int g = 0; g < 2; ++g) vw[2 * p + g] = ic_voff(lane, g, 6144) + (2 * w + g) * 8 * 6144u + p * 2048u + ct * 128u;
#pragma unroll
    for (int g = 0; g < 4; ++g) vx[g] = kc_voff(lane, g & 1, 2048) + (s0 + (4 * w + g) * 8) * 2048u;
  }
  DI void issue(unsigned slot0, int w, int t) {
    const unsigned sw = slot0 + w * 2048, sx = slot0 + 24576 + w * 4096, ow = t * (64u * 6144u), ox = t * 128;
    piece<0, false>(sw, vw[0], rw, ow); piece<1024, false>(sw, vw[1], rw, ow); piece<8192, false>(sw, vw[2], rw, ow);
    piece<9216, false>(sw, vw[3], rw, ow); piece<16384, false>(sw, vw[4], rw, ow); piece<17408, false>(sw, vw[5], rw, ow);
    piece<0, false>(sx, vx[0], rx, ox); piece<1024, false>(sx, vx[1], rx, ox); piece<2048, false>(sx, vx[2], rx, ox); piece<3072, false>(sx, vx[3], rx, ox);
  }
};
struct KVStream {
  rsrc_t rk, rv; unsigned vk, vv;
  DI void init(const Args& a, int lane, int w, int head) {
    rk = make_rsrc(a.Q + (size_t)(32 + head) * 65536, 65536 * 2 + 4096); rv = make_rsrc(a.Q + (size_t)(64 + head) * 65536, 65536 * 2);
    vk = k_voff(lane) + w * 1024; vv = v_voff(lane) + w * 8 * 4096;
  }
  DI void issue(unsigned slot0, int w, int t) { const unsigned sb = slot0 + w * 1024; piece<32768, true>(sb, vk, rk, t * 4096 + 2048); piece<36864, true>(sb, vv, rv, t * 128); }
  DI void issue_first(unsigned slot0, int w) { const unsigned sb = slot0 + w * 1024; piece<32768, true>(sb, vk, rk, 0); piece<36864, true>(sb, vk, rk, 4096); }
};
struct T5Stream {
  rsrc_t rw, ro; unsigned vw[4], vo[2];
  DI void init(const Args& a, int lane, int w, unsigned n0, unsigned s5) {
    rw = make_rsrc(a.wo, 1024 * 1024 * 2); ro = make_rsrc(a.O, 2048 * 1024 * 2);
#pragma unroll
    for (int p = 0; p < 2; ++p)
#pragma unroll
      for (int g = 0; g < 2; ++g) vw[2 * p + g] = ic_voff(lane, g, 2048) + (2 * w + g) * 8 * 2048u + (n0 + p * 64) * 2u;
#pragma unroll
    for (int g = 0; g < 2; ++g) vo[g] = kc_voff(lane, g, 2048) + (s5 + (2 * w + g) * 8) * 2048u;
  }
  DI void issue(unsigned slot0, int w, int t) {
    const unsigned sb = slot0 + w * 2048, ow = t * (64u * 2048u), oo = t * 128;
    piece<0, false>(sb, vw[0], rw, ow); piece<1024, false>(sb, vw[1], rw, ow); piece<8192, false>(sb, vw[2], rw, ow); piece<9216, false>(sb, vw[3], rw, ow);
    piece<16384, true>(sb, vo[0], ro, oo); piece<17408, true>(sb, vo[1], ro, oo);
  }
};
constexpr unsigned OOB = 0x7ff00000u;
DI void l2warm(unsigned voff, rsrc_t rs, unsigned soff) { asm volatile("buffer_load_dword v167, %0, %1, %2 offen sc1" ::"v"(voff), "s"(rs), "s"(soff) : "memory", "v167"); }
struct Warm {
  rsrc_t rs; unsigned v, dv; u64 mask;
  DI void fire() { l2warm(v, rs, 0u); v += dv; }
  DI void step(int s) { const bool act = s < 64 && ((mask >> s) & 1); l2warm(v, rs, act ? 0u : OOB); if (act) v += dv; }
};
constexpr u64 bit_(int s) { return 1ull << s; }
constexpr __host__ __device__ bool in_(int s, int a, int n) { return s >= a && s < a + n; }
constexpr __host__ __device__ int npieces(int s) { return (s < 16 ? 10 : 0) + ((in_(s, S_T2, 16) || in_(s, S_T4, 32)) ? 8 : 0) + (in_(s, S_A, 32) ? 2 : 0) + (in_(s, S_T5, 16) ? 6 : 0); }
constexpr __host__ __device__ int npf(int s) { return s == S_WO1 ? 2 : 1; }
constexpr __host__ __device__ int nwait(int s) { return npieces(s + 1) + npieces(s + 2) + (s == S_A - 1 ? 0 : npf(s - 3)) + npf(s - 2) + npf(s - 1); }
constexpr __host__ __device__ int run_end(int s0, int s1) { int lo = nwait(s0), hi = lo, e = s0 + 1;
  for (; e < s1; ++e) { const int n = nwait(e), l = n < lo ? n : lo, h = n > hi ? n : hi; if (h - l > 2) break; lo = l; hi = h; }
  return e; }
constexpr __host__ __device__ int run_wait(int s0, int e) { int lo = nwait(s0); for (int s = s0; s < e; ++s) if (nwait(s) < lo) lo = nwait(s); return lo; }
template <int S0, int S1, class F> DI void for_steps(F&& f) {
  if constexpr (S0 < S1) {
    constexpr int E = run_end(S0, S1), W = run_wait(S0, E);
#pragma nounroll
    for (int s = S0; s < E; ++s) { wait_vm<W>(); barrier(); f(s); }
    for_steps<E, S1>(f);
  }
}
DI void role_dma(const Args& a, const Ids& id) {
  const int lane = id.lane, w = id.wid - 8, xcd = id.xcd, idx = id.idx;
  const int rl = idx & 7, cl = idx >> 3, head = xcd * 4 + cl, half = xcd & 1, tb = idx & 3, eb = idx >> 2;
  const unsigned s0 = ((xcd & 1) * 8 + rl) * 128, ct = (xcd >> 1) * 4 + cl, eg = ct * 128;
  const unsigned t0 = (xcd >> 1) * 512 + tb * 128, e0 = eb * 128;
  const unsigned n0 = ((xcd & 1) * 4 + tb) * 128, s5 = ((xcd >> 1) * 8 + eb) * 64;
  const int ph1 = (rl + cl) & 3, ph4 = (tb + eb) & 1;
  auto slot_of = [&](int s) { return id.lds0 + (s & 3) * SLOT; };
  T1Stream t1; t1.init(a, lane, w, s0, ct);
  t1.issue(slot_of(0), w, 0);
  Warm f;
  const unsigned vx = (s0 + 32 * cl + (lane & 31)) * 2048u + ((lane >> 5) + 2 * w) * 128u;
  { const int wl = (w & 1) * 64 + lane;
    f.rs = w < 2 ? make_rsrc(a.Ax, 2048 * 1024 * 2) : make_rsrc(a.wqkv, 1024 * 3072 * 2);
    f.v = w < 2 ? vx : (wl < 96 ? (32 * rl + wl / 3) * 6144u + (wl % 3) * 2048u + ct * 128u : OOB);
    f.dv = w < 2 ? 512u : (wl < 96 ? 256u * 6144u : 0u); }
  f.fire();
  wait_vm<1>();
  barrier();
  t1.issue(slot_of(1), w, 1); t1.issue(slot_of(2), w, 2); t1.issue(slot_of(3), w, 3);
  f.fire();
  f.mask = (bit_(0) | bit_(4)) << ph1; f.step(0);
  l2warm(OOB, f.rs, OOB);
  for_steps<1, 8>([&](int s) { t1.issue(slot_of(s + 3), w, s + 3); f.step(s); });
  const void* wd = eg >= 1024 ? a.Wi : a.Wr;
  f.rs = w < 2 ? make_rsrc(a.Bx, 2048 * 1024 * 2) : make_rsrc(wd, 1024 * 1024 * 2);
  f.v = w < 2 ? vx : (w == 2 ? ((eg & 1023) + 16 * rl + (lane & 15)) * 2048u + (lane >> 4) * 128u : OOB);
  f.dv = w < 3 ? 512u : 0u;
  f.mask = (bit_(13) | bit_(14) | bit_(S_T2 + 1) | bit_(S_T2 + 5)) << (ph1 & 1);
  for_steps<8, 13>([&](int s) { t1.issue(slot_of(s + 3), w, s + 3); f.step(s); });
  KKStream<false> g2; g2.init(lane, w, a.Bx, 2048 * 1024 * 2, s0, wd, 1024 * 1024 * 2, eg & 1023, 2048);
  for_steps<13, S_T2 - 3>([&](int s) { f.step(s); });
  for_steps<S_T2 - 3, S_A - 3>([&](int s) { g2.issue(slot_of(s + 3), w, s + 3 - S_T2); f.step(s); });
  KVStream kv; kv.init(a, lane, w, head);
  if (w == 0) { f.rs = make_rsrc(half ? a.Ci : a.Cr, 2048 * 2048 * 2); f.v = (t0 + 16 * eb + (lane & 15)) * 4096u + (lane >> 4) * 128u; f.dv = 512;
    f.mask = (bit_(S_T2 + 11) | bit_(S_T2 + 13) | bit_(S_T4 - 3) | bit_(S_T4 + 1) | bit_(S_T4 + 4) | bit_(S_T4 + 7) | bit_(S_T4 + 10) | bit_(S_T4 + 13)) << ph4; }
  else if (w < 3) { f.rs = make_rsrc(a.ABt + (size_t)half * 1024 * 2048, 1024 * 2048 * 2);
    f.v = (e0 + 32 * tb + (lane & 31)) * 4096u + ((lane >> 5) + 2 * (w - 1)) * 128u + 512; f.dv = 512;
    f.mask = bit_(S_T4 - 3) | ((bit_(S_T4 - 1) | bit_(S_T4 + 2) | bit_(S_T4 + 5) | bit_(S_T4 + 8) | bit_(S_T4 + 11) | bit_(S_T4 + 14)) << ph4); }
  else { f.rs = make_rsrc(a.Q + (size_t)(32 + head) * 65536, 33 * 65536 * 2);
    f.v = lane < 16 ? 2048u + (rl * 16 + lane) * 128u + 16384u : (lane < 32 ? 32u * 65536 * 2 + (rl * 4 + ((lane >> 2) & 3)) * 4096u + (lane & 3) * 128u + 512u : OOB);
    f.dv = lane < 16 ? 16384u : (lane < 32 ? 512u : 0u);
    f.mask = bit_(S_A - 3) | ((bit_(S_A - 1) | bit_(S_A + 2) | bit_(S_A + 5) | bit_(S_A + 12) | bit_(S_A + 16) | bit_(S_A + 20)) << (idx & 1)); }
  for_steps<S_A - 3, S_A - 2>([&](int s) { kv.issue_first(slot_of(S_A - 1), w); g2.issue(slot_of(s + 3), w, s + 3 - S_T2); kv.issue(slot_of(s + 3), w, s + 3 - S_A); f.step(s); });
  for_steps<S_A - 2, S_T2 + 13>([&](int s) { g2.issue(slot_of(s + 3), w, s + 3 - S_T2); kv.issue(slot_of(s + 3), w, s + 3 - S_A); f.step(s); });
  for_steps<S_T2 + 13, S_T4 - 3>([&](int s) { kv.issue(slot_of(s + 3), w, s + 3 - S_A); f.step(s); });
  KKStream<true> g4; g4.init(lane, w, a.ABt + (size_t)half * 1024 * 2048, 1024 * 2048 * 2, e0, half ? a.Ci : a.Cr, 2048 * 2048 * 2, t0, 4096);
  Warm fw; fw.rs = make_rsrc(a.wo, 1024 * 1024 * 2); fw.v = (128 * eb + (w & 1) * 64 + lane) * 2048u + n0 * 2 + (w >> 1) * 128u; fw.dv = 0;
  for_steps<S_T4 - 3, S_A + 29>([&](int s) { g4.issue(slot_of(s + 3), w, s + 3 - S_T4); kv.issue(slot_of(s + 3), w, s + 3 - S_A); f.step(s); if (s == S_WO1) fw.fire(); });
  for_steps<S_A + 29, S_T5 - 3>([&](int s) { g4.issue(slot_of(s + 3), w, s + 3 - S_T4); if (s == S_WO2) fw.fire(); else f.step(s); });
  T5Stream t5; t5.init(a, lane, w, n0, s5);
  for_steps<S_T5 - 3, S_END - 3>([&](int s) { t5.issue(slot_of(s + 3), w, s + 3 - S_T5); f.step(s); });
  for_steps<S_END - 3, S_END>([&](int s) { f.step(s); });
}

DI void role_g0(const Args& a, const Ids& id) {
  const int wid = id.wid, wi = wid & 1, wj = wid >> 1;
  u64 ep = load_epoch(a);
  {
    const int lane = id.lane, rl = id.idx & 7, cl = id.idx >> 3, rb = (id.xcd & 1) * 8 + rl, ct = (id.xcd >> 1) * 4 + cl;
    const int s0 = rb * 128 + 64 * wj, head = 2 * ct + wi;
    Cons<6, 4, true, 4, true> c;
#pragma unroll
    for (int i = 0; i < 6; ++i) c.pa[i] = id.lds0 + ic_raddr(lane, 4 * (i >> 1) + 2 * wi + (i & 1));
#pragma unroll
    for (int k = 0; k < 2; ++k) c.qa[k] = id.lds0 + 24576 + kc_raddr(lane, 64 * wj, k);
    c.run<16>(0);
    barrier();
    ep = uniform64(ep);
    const int ln = fresh_lane();
    const unsigned stg = id.lds0 + wj * SLOT + 32768 + wi * 4096;
    const rsrc_t rq = make_rsrc(a.Q, 3u * 32 * 65536 * 2);
    const unsigned oq = (unsigned)(head * 65536 + s0 * 32) * 2u, ok = oq + 32 * 65536 * 2;
    u32x4 r[4];
    prep64<true>(c.acc[0][0], c.acc[1][0], c.acc[0][1], c.acc[1][1], QSCALE, stg, ln, r);
    prep64<true>(c.acc[0][2], c.acc[1][2], c.acc[0][3], c.acc[1][3], QSCALE, stg + 2048, ln, r + 2);
    barrier();
    commit64<true>(r, ln, rq, oq, 64); commit64<true>(r + 2, ln, rq, oq + 2048, 64);
    prep64<false>(c.acc[2][0], c.acc[3][0], c.acc[2][1], c.acc[3][1], 1.0f, stg, ln, r);
    prep64<false>(c.acc[2][2], c.acc[3][2], c.acc[2][3], c.acc[3][3], 1.0f, stg + 2048, ln, r + 2);
    barrier();
    commit64<true>(r, ln, rq, ok, 64); commit64<true>(r + 2, ln, rq, ok + 2048, 64);
    {
      const int dl = ln & 15, g = ln >> 4, pos = (g & 1) * 2 + (g >> 1);
#pragma unroll
      for (int db = 0; db < 2; ++db)
#pragma unroll
        for (int jj = 0; jj < 4; ++jj) { const f32x4& v = c.acc[4 + db][jj]; const int dd = 16 * db + dl;
          lds_wr64(stg + dd * 128 + (((2 * jj + (pos >> 1)) ^ (dd & 7)) * 16) + (pos & 1) * 8, u32x2{cvtpk(v[0], v[1]), cvtpk(v[2], v[3])}); }
      read128(stg, ln, r);
    }
    barrier();
    commit128<true>(r, ln, rq, (unsigned)((64 + head) * 65536 + s0) * 2u, 4096);
    barrier();
    signal(a.flg, F_QKV + ct * 64 + rb * 4 + wid, ep, ln);
    idle(S_A - 3 - 21);
    if (wid == 0) await(a.flg, F_QKV + ((id.xcd * 4 + cl) >> 1) * 64, 64, ep, ln);
    barrier();
  }
  {
    const int lane = fresh_lane(), r = lane & 31, h = lane >> 5;
    const int head = id.xcd * 4 + (id.idx >> 3), qb = id.idx & 7, q0 = qb * 256 + wid * 64;
    const rsrc_t rq = make_rsrc(a.Q + (size_t)head * 65536, 65536 * 2);
    AttnWave w;
#pragma unroll
    for (int b = 0; b < 2; ++b)
#pragma unroll
      for (int s = 0; s < 2; ++s) w.q[b][s] = as_bf(ld16s(rq, (q0 + 32 * b + r) * 64 + 32 * s + 16 * h, 0));
    idle(2);
    const unsigned kp = id.lds0 + ((S_A - 1) & 3) * SLOT + 32768;
    bf16x8 k0[4][2];
#pragma unroll
    for (int j = 0; j < 4; ++j) { k0[j][0] = lds_rd128(kp + k_raddr(lane, 0) + j * 2048); k0[j][1] = lds_rd128(kp + k_raddr(lane, 1) + j * 2048); }
#pragma unroll
    for (int b = 0; b < 2; ++b) {
      float mx = -INFINITY;
#pragma unroll
      for (int j = 3; j >= 0; --j) {
        f32x16 s = mfma32(k0[j][0], w.q[b][0], f32x16{}); s = mfma32(k0[j][1], w.q[b][1], s);
#pragma unroll
        for (int i = 0; i < 16; ++i) mx = __builtin_fmaxf(mx, s[i]);
        if (j == 0 && b == 0) w.S[0] = s;
      }
      const float nm = -(swap32_max(mx) + MARGIN);
#pragma unroll
      for (int i = 0; i < 16; ++i) w.NM[b][i] = nm;
      if (b == 0) w.S[0] = w.S[0] + nm;
      w.O[b] = f32x16{}; w.L[b] = f32x4{};
    }
    w.kf[0] = k0[0][0]; w.kf[1] = k0[0][1];
    w.S[1] = f32x16{};
    w.P[1][0] = u32x4{}; w.P[1][1] = u32x4{}; w.P[0][0] = u32x4{}; w.P[0][1] = u32x4{};
    w.vf[0] = bf16x8{}; w.vf[1] = bf16x8{};
    {
      const int m = lane & 15; const __bf16 one = (__bf16)1.0f, z = (__bf16)0.0f; const bool lo = (m == 0 || m == 4), hi = (m == 8 || m == 12);
      w.asel = bf16x8{lo ? one : z, lo ? one : z, lo ? one : z, lo ? one : z, hi ? one : z, hi ? one : z, hi ? one : z, hi ? one : z};
      w.sidx = (m == 4 || m == 12) ? 0xEEEE : 0x4444; }
    constexpr unsigned so = (S_A & 3) * SLOT;
    w.ka0 = id.lds0 + so + 32768 + k_raddr(lane, 0); w.ka1 = id.lds0 + so + 32768 + k_raddr(lane, 1);
    w.va0 = id.lds0 + so + 36864 + v_raddr(lane, 0); w.va1 = id.lds0 + so + 36864 + v_raddr(lane, 1);
    w.run(S_A & 3);
    barrier();
    const int ln = fresh_lane(), r2 = ln & 31, h2 = ln >> 5;
    const unsigned stg = id.lds0 + ((S_A + 32 + wj) & 3) * SLOT + 32768 + wi * 4096;
    u32x4 ro[2];
#pragma unroll
    for (int b = 0; b < 2; ++b) {
      const int src = (r2 & 15) + (r2 >> 4) * 32;
      float l = __shfl(w.L[b][0], src) + __shfl(w.L[b][0], src + 16);
      float amax = 0.f;
#pragma unroll
      for (int i = 0; i < 16; ++i) amax = __builtin_fmaxf(amax, __builtin_fabsf(w.O[b][i]));
      bool nan = false;
#pragma unroll
      for (int i = 0; i < 16; ++i) nan |= !(w.O[b][i] == w.O[b][i]);
      const bool bad = !(l < 0x1p126f) || !(amax < 0x1p126f) || nan || (amax < 0x1p-115f);
      if (__any(bad)) attn_exact(make_rsrc(a.Q + (size_t)(32 + head) * 65536, 65536 * 2), make_rsrc(a.Q + (size_t)(64 + head) * 65536, 65536 * 2), w.q[b], ln, w.O[b], l);
      const float inv = 1.0f / l;
      const f32x16 v = w.O[b] * inv;
      lds_wr128(stg + b * 2048 + r2 * 64 + ((h2 ^ ((r2 >> 1) & 3)) * 16), u32x4{cvtpk(v[0], v[1]), cvtpk(v[2], v[3]), cvtpk(v[4], v[5]), cvtpk(v[6], v[7])});
      lds_wr128(stg + b * 2048 + r2 * 64 + (((2 + h2) ^ ((r2 >> 1) & 3)) * 16), u32x4{cvtpk(v[8], v[9]), cvtpk(v[10], v[11]), cvtpk(v[12], v[13]), cvtpk(v[14], v[15])});
#pragma unroll
      for (int m = 0; m < 2; ++m) { const int row = 32 * b + 16 * m + (ln >> 2), c = ln & 3; ro[m] = lds_rdu(stg + row * 64 + ((c ^ ((row >> 1) & 3)) * 16)); }
      barrier();
      commit64<true>(ro, ln, make_rsrc(a.O, 2048 * 1024 * 2), (unsigned)((q0 + 32 * b) * 1024 + head * 32) * 2u, 2048);
    }
    barrier();
    signal(a.flg, F_O + qb * 128 + head * 4 + wid, ep, ln);
    idle(S_T5 - 3 - (S_A + 36));
    if (wid == 0) {
      await(a.flg, F_O + ((id.xcd >> 1) * 2 + (id.idx >> 4)) * 128, 128, ep, ln);
      if (blockIdx.x == 0 && ln == 0) __hip_atomic_store(a.flg + F_EPOCH, ep, __ATOMIC_RELAXED, __HIP_MEMORY_SCOPE_AGENT);
    }
    idle(3);
  }
  {
    const int lane = fresh_lane();
    const int n0 = ((id.xcd & 1) * 4 + (id.idx & 3)) * 128, s5 = ((id.xcd >> 1) * 8 + (id.idx >> 2)) * 64;
    Cons<4, 2, true> c;
    constexpr unsigned so = (S_T5 & 3) * SLOT;
#pragma unroll
    for (int i = 0; i < 4; ++i) c.pa[i] = id.lds0 + so + ic_raddr(lane, 4 * wi + i);
#pragma unroll
    for (int k = 0; k < 2; ++k) c.qa[k] = id.lds0 + so + 16384 + kc_raddr(lane, 32 * wj, k);
    c.run<16>(S_T5 & 3);
    const int ln = fresh_lane();
    u32x4 r[4];
    prep128(c.acc, 0, 0u, id.lds0 + (2 + wj) * SLOT + 32768 + wi * 4096, ln, r);
    commit128<false>(r, ln, make_rsrc(a.out, 2048 * 2048 * 2), (unsigned)((s5 + 32 * wj) * 2048 + n0 + 64 * wi) * 2u, 4096);
  }
}

DI void role_g1(const Args& a, const Ids& id) {
  const int wi = id.wid & 1, wj = (id.wid >> 1) & 1, xcd = id.xcd, idx = id.idx;
  u64 ep = load_epoch(a);
  {
    const int lane = id.lane, rb = (xcd & 1) * 8 + (idx & 7), ct = (xcd >> 1) * 4 + (idx >> 3);
    const int s0 = rb * 128, eg = ct * 128;
    Cons<4, 4, false> c;
    constexpr unsigned so = (S_T2 & 3) * SLOT;
#pragma unroll
    for (int k = 0; k < 2; ++k) { c.pa[k] = id.lds0 + so + kc_raddr(lane, 64 * wi, k); c.qa[k] = id.lds0 + so + 16384 + kc_raddr(lane, 64 * wj, k); }
    idle(8);
    ep = uniform64(ep);
    idle(S_T2 - 8);
    c.run<16>(S_T2 & 3);
    barrier();
    const int ln = fresh_lane();
    const unsigned stg = id.lds0 + so + (id.wid & 3) * 4096;
    const unsigned sgn = eg >= 1024 ? 0x80008000u : 0u;
    const rsrc_t rab = make_rsrc(a.ABt, 2048u * 2048 * 2);
    u32x4 r[4];
    prep128(c.acc, 0, sgn, stg, ln, r);
    barrier();
    commit128<true>(r, ln, rab, (unsigned)((eg + 64 * wj) * 2048 + s0 + 64 * wi) * 2u, 4096);
    prep128(c.acc, 1, sgn, stg, ln, r);
    barrier();
    commit128<true>(r, ln, rab, (unsigned)((eg + 64 * wj + 32) * 2048 + s0 + 64 * wi) * 2u, 4096);
    barrier();
    signal(a.flg, F_AB + ct * 64 + rb * 4 + (id.wid & 3), ep, ln);
    idle(S_T4 - 3 - (S_T2 + 20));
    if (id.wid == 4) await(a.flg, F_AB + ((xcd & 1) * 8 + (idx >> 2)) * 64, 64, ep, ln);
    idle(3);
  }
  {
    const int lane = fresh_lane(), half = xcd & 1, t0 = (xcd >> 1) * 512 + (idx & 3) * 128, e0 = (idx >> 2) * 128;
    Cons<4, 4, false> c;
    constexpr unsigned so = (S_T4 & 3) * SLOT;
#pragma unroll
    for (int k = 0; k < 2; ++k) { c.pa[k] = id.lds0 + so + kc_raddr(lane, 64 * wi, k); c.qa[k] = id.lds0 + so + 16384 + kc_raddr(lane, 64 * wj, k); }
    c.run<32>(S_T4 & 3);
    barrier();
    const int ln = fresh_lane();
    if (wj != half) {
      const rsrc_t rx = make_rsrc(a.X + (size_t)(blockIdx.x * 2 + wi) * 4096, 8192);
#pragma unroll
      for (int m = 0; m < 8; ++m) { const f32x4 &u = c.acc[m >> 1][(2 * m) & 3], &v = c.acc[m >> 1][(2 * m + 1) & 3];
        st16<true>(rx, ln * 16, m * 1024, u32x4{cvtpk(u[0], u[1]), cvtpk(u[2], u[3]), cvtpk(v[0], v[1]), cvtpk(v[2], v[3])}); }
      idle(3);
      signal(a.flg, F_P + (blockIdx.x ^ 1) * 2 + wi, ep, ln);
      return;
    }
    idle(9);
    await(a.flg, F_P + blockIdx.x * 2 + wi, 1, ep, ln);
    const rsrc_t rx = make_rsrc(a.X + (size_t)((blockIdx.x ^ 1) * 2 + wi) * 4096, 8192);
    u32x4 y[8];
#pragma unroll
    for (int m = 0; m < 8; ++m) y[m] = ld16s(rx, ln * 16, m * 1024);
    idle(6);
#pragma unroll
    for (int m = 0; m < 8; ++m)
#pragma unroll
      for (int e = 0; e < 2; ++e) { f32x4& u = c.acc[m >> 1][(2 * m + e) & 3];
        u[0] += __uint_as_float(y[m][2 * e] << 16); u[1] += __uint_as_float(y[m][2 * e] & 0xffff0000u);
        u[2] += __uint_as_float(y[m][2 * e + 1] << 16); u[3] += __uint_as_float(y[m][2 * e + 1] & 0xffff0000u); }
    const unsigned stg = id.lds0 + 32768 + wi * 4096;
    const rsrc_t ro = make_rsrc(a.out, 2048 * 2048 * 2);
#pragma unroll
    for (int p = 0; p < 2; ++p) {
      u32x4 r[4]; prep128(c.acc, p, 0u, stg, ln, r);
      commit128<false>(r, ln, ro, (unsigned)((t0 + 64 * wj + 32 * p) * 2048 + 1024 + e0 + 64 * wi) * 2u, 4096);
    }
  }
}

__global__ __launch_bounds__(768, 3) void fused_mha_fnet_kernel(Args a) {
  extern __shared__ __attribute__((aligned(16))) char lds[];
  Ids id; id.lane = threadIdx.x & 63; id.wid = __builtin_amdgcn_readfirstlane(threadIdx.x >> 6);
  id.xcd = blockIdx.x & 7; id.idx = blockIdx.x >> 3;
  id.lds0 = (unsigned)(size_t)(LAS char*)lds;
  if (id.wid < 4) role_g0(a, id);
  else if (id.wid < 8) role_g1(a, id);
  else role_dma(a, id);
}

extern "C" void kernel_launch(void* const* d_in, const int* in_sizes, int n_in, void* d_out, int out_size, void* d_ws, size_t ws_size, hipStream_t stream) {
  u16* ws = (u16*)d_ws; const size_t M = (size_t)2048 * 1024;
  Args a;
  a.Ax = (const u16*)d_in[0]; a.wqkv = (const u16*)d_in[1]; a.wo = (const u16*)d_in[2]; a.Bx = (const u16*)d_in[3];
  a.Cr = (const u16*)d_in[4]; a.Ci = (const u16*)d_in[5]; a.Wr = (const u16*)d_in[6]; a.Wi = (const u16*)d_in[7];
  a.Q = ws; a.ABt = ws + 3 * M; a.O = ws + 5 * M; a.X = ws + 6 * M; a.out = (u16*)d_out; a.flg = (u64*)(ws + 8 * M);
  (void)hipFuncSetAttribute((const void*)fused_mha_fnet_kernel, hipFuncAttributeMaxDynamicSharedMemorySize, LDS_ALL);
  fused_mha_fnet_kernel<<<256, 768, LDS_ALL, stream>>>(a);
}
```

```cpp
#include <hip/hip_runtime.h>
#include <type_traits>

typedef unsigned short u16;
typedef unsigned long long u64;
typedef __bf16 bf16x8 __attribute__((ext_vector_type(8)));
typedef __bf16 bf16x16 __attribute__((ext_vector_type(16)));
typedef __bf16 bf16x2 __attribute__((ext_vector_type(2)));
typedef short s16x4 __attribute__((ext_vector_type(4)));
typedef short s16x8 __attribute__((ext_vector_type(8)));
typedef float f32x2 __attribute__((ext_vector_type(2)));
typedef float f32x4 __attribute__((ext_vector_type(4)));
typedef float f32x16 __attribute__((ext_vector_type(16)));
typedef unsigned u32x4 __attribute__((ext_vector_type(4)));
typedef unsigned u32x2 __attribute__((ext_vector_type(2)));
typedef unsigned u32x8 __attribute__((ext_vector_type(8)));
typedef __amdgpu_buffer_rsrc_t rsrc_t;
#define LAS __attribute__((address_space(3)))
#define DI __device__ __forceinline__
#define SBAR() __builtin_amdgcn_sched_barrier(0)
#define PIN(x) asm volatile("" : "+v"(x))

constexpr int SLOT = 40960, LDS_ALL = 4 * SLOT;
constexpr int S_T2 = 19;
constexpr int S_A = 27;
constexpr int S_T4 = S_T2 + 25;
constexpr int S_T5 = S_T4 + 32;
constexpr int S_END = S_T5 + 16;
constexpr int S_WO1 = S_T4 + 3, S_WO2 = S_T4 + 25;
static_assert(S_T2 >= 17 && S_A - 3 >= 22 && S_A - 2 <= S_T2 + 13 && S_T4 - 3 >= S_T2 + 21 && S_T4 - 2 <= S_A + 29 && S_T5 - 3 >= S_A + 36 &&
              S_WO1 < S_A + 29 && S_WO2 >= S_A + 29 && S_WO2 < S_T5 - 3 && S_T4 + 15 < 64, "schedule");
constexpr float QSCALE = 0.17677669529663687f * 1.4426950408889634f;
constexpr float MARGIN = 100.0f;

struct Args {
  const u16 *Ax, *wqkv, *wo, *Bx, *Cr, *Ci, *Wr, *Wi;
  u16 *Q;
  u16 *ABt;
  u16 *O;
  u16 *X;
  u16 *out;
  u64 *flg;
};
constexpr int F_QKV = 0, F_AB = 1024, F_O = 2048, F_P = 3072, F_EPOCH = 3600, F_TMO = 3632;
struct Ids { int lane, wid, xcd, idx; unsigned lds0; };

DI int fresh_lane() { int l; asm volatile("v_mbcnt_lo_u32_b32 %0, -1, 0\n\tv_mbcnt_hi_u32_b32 %0, -1, %0" : "=v"(l)); return l; }
DI rsrc_t make_rsrc(const void* p, unsigned bytes) { return __builtin_amdgcn_make_buffer_rsrc((void*)p, 0, bytes, 0x00020000); }
DI u32x4 ld16s(rsrc_t rs, unsigned voff, unsigned soff) { return __builtin_amdgcn_raw_buffer_load_b128(rs, voff, soff, 16); }
DI void st16(rsrc_t rs, unsigned voff, unsigned soff, u32x4 v) { __builtin_amdgcn_raw_buffer_store_b128(v, rs, voff, soff, 16); }
template <int N> DI void wait_vm() { asm volatile("s_waitcnt vmcnt(%0)" ::"n"(N) : "memory"); }
DI void barrier() { asm volatile("s_barrier" ::: "memory"); }
DI void sync_lds() { asm volatile("s_waitcnt lgkmcnt(0)\n\ts_barrier" ::: "memory"); }
DI void idle(int n) {
#pragma nounroll
  for (int i = 0; i < n; ++i) barrier();
}
DI unsigned cvtpk(float lo, float hi) { f32x2 v = {lo, hi}; bf16x2 b = __builtin_convertvector(v, bf16x2); return __builtin_bit_cast(unsigned, b); }
DI f32x16 mfma32(bf16x8 a, bf16x8 b, f32x16 c) { return __builtin_amdgcn_mfma_f32_32x32x16_bf16(a, b, c, 0, 0, 0); }
DI f32x4 mfma16(bf16x8 a, bf16x8 b, f32x4 c) { return __builtin_amdgcn_mfma_f32_16x16x32_bf16(a, b, c, 0, 0, 0); }
DI bf16x8 lds_rd128(unsigned addr) { return *(const LAS bf16x8*)(size_t)addr; }
DI u32x4 lds_rdu(unsigned addr) { return *(const LAS u32x4*)(size_t)addr; }
DI void lds_wr64(unsigned addr, u32x2 v) { *(LAS u32x2*)(size_t)addr = v; }
DI void lds_wr128(unsigned addr, u32x4 v) { *(LAS u32x4*)(size_t)addr = v; }
DI s16x4 lds_rdtr(unsigned addr) { return __builtin_amdgcn_ds_read_tr16_b64_v4i16((LAS s16x4*)(size_t)addr); }
DI bf16x8 cat8(s16x4 a, s16x4 b) { s16x8 r = __builtin_shufflevector(a, b, 0, 1, 2, 3, 4, 5, 6, 7); return __builtin_bit_cast(bf16x8, r); }
DI bf16x16 cat16(u32x4 a, u32x4 b) { u32x8 r = __builtin_shufflevector(a, b, 0, 1, 2, 3, 4, 5, 6, 7); return __builtin_bit_cast(bf16x16, r); }
DI bf16x8 as_bf(u32x4 v) { return __builtin_bit_cast(bf16x8, v); }
DI float ex2(float x) { return __builtin_amdgcn_exp2f(x); }

DI u64 load_epoch(const Args& a) { return __hip_atomic_load(a.flg + F_EPOCH, __ATOMIC_RELAXED, __HIP_MEMORY_SCOPE_AGENT) + 1; }
DI u64 uniform64(u64 v) { unsigned lo = __builtin_amdgcn_readfirstlane((unsigned)v), hi = __builtin_amdgcn_readfirstlane((unsigned)(v >> 32)); asm volatile("" : "+s"(lo), "+s"(hi)); return lo | ((u64)hi << 32); }
DI void signal(u64* flg, int word, u64 e, int lane) { wait_vm<0>(); if (lane == 0) __hip_atomic_store(flg + word, e, __ATOMIC_RELAXED, __HIP_MEMORY_SCOPE_AGENT); }
DI void await(u64* flg, int word0, int n, u64 e, int lane) {
  u64* p0 = flg + word0 + (lane < n ? lane : 0); u64* p1 = flg + word0 + (lane + 64 < n ? lane + 64 : 0);
  const u64 t0 = __builtin_amdgcn_s_memrealtime();
  for (;;) {
    const u64 v0 = __hip_atomic_load(p0, __ATOMIC_RELAXED, __HIP_MEMORY_SCOPE_AGENT), v1 = __hip_atomic_load(p1, __ATOMIC_RELAXED, __HIP_MEMORY_SCOPE_AGENT);
    if (__all(v0 == e && v1 == e)) break;
    if (__builtin_amdgcn_s_memrealtime() - t0 > 100000000ull) { if (lane == 0) __hip_atomic_store(flg + F_TMO, e, __ATOMIC_RELAXED, __HIP_MEMORY_SCOPE_AGENT); break; }
    __builtin_amdgcn_s_sleep(1);
  }
}

DI unsigned kc_voff(int lane, int par, unsigned ldb) { return (unsigned)(lane >> 3) * ldb + (unsigned)(((lane & 7) ^ (((lane >> 4) + 4 * par) & 7)) * 16); }
DI unsigned ic_voff(int lane, int par, unsigned ldb) { const int c = lane & 7, u = (c >> 1) ^ (((lane >> 4) & 1) << 1) ^ par; return (unsigned)(lane >> 3) * ldb + (unsigned)((u * 2 + (c & 1)) * 16); }
DI unsigned kc_raddr(int lane, int row0, int ks) { const int row = row0 + (lane & 15); return (unsigned)(row * 128 + (((4 * ks + (lane >> 4)) ^ ((row >> 1) & 7)) * 16)); }
DI unsigned ic_raddr(int lane, int cb) {
  const int g = lane >> 4, q = (lane & 15) >> 2, p = lane & 3, m = (((q >> 1) & 1) << 1) ^ (g & 1);
  return (unsigned)((cb >> 2) * 8192 + (8 * g + q) * 128 + (((cb & 3) ^ m) * 32) + p * 8);
}

template <int NI, int NJ, bool PIC, int SWAPI = 99, bool ROLLP = false> struct Cons {
  static constexpr int NPA = PIC ? NI : 2, NM = NI * NJ, PI = PIC ? 2 : 1, NIT = NJ + NI * PI;
  static constexpr int RG = NM - 1 < NIT ? NM - 1 : NIT;
  unsigned pa[NPA], qa[2];
  f32x4 acc[NI][NJ];
  bf16x8 p0[NI], q0[NJ], p1[NI], q1[NJ];
  s16x4 tlo;
  template <int KS, int K> DI void rd(bf16x8 (&p)[NI], bf16x8 (&q)[NJ]) {
    if constexpr (K == 0) q[0] = lds_rd128(qa[KS]);
    else if constexpr (K > PI && K < PI + NJ) q[K - PI] = lds_rd128(qa[KS] + (K - PI) * 2048);
    else { constexpr int r = K <= PI ? K - 1 : K - NJ, i = r / PI, hf = r % PI;
      if constexpr (!PIC) p[i] = lds_rd128(pa[KS] + i * 2048);
      else if constexpr (hf == 0) tlo = lds_rdtr(pa[i] + KS * 4096);
      else p[i] = cat8(tlo, lds_rdtr(pa[i] + KS * 4096 + 512)); }
  }
  static constexpr int free_gap(int R) {
    if (ROLLP) return R < NPA ? (R < NI - 1 ? (R + 1) * NJ : NM - 1) : (R == NPA ? 0 : 5);
    if (R == NPA) return PIC ? 0 : 1;
    if (!PIC && R == 0) return 0;
    const int g = RG + (R < NPA ? (PIC ? R + 1 : 1) : 0) / 2;
    return g < NM ? g : NM - 1;
  }
  template <int M, int R> DI void adv_at(int delta) {
    if constexpr (R < NPA + 2) {
      if constexpr (free_gap(R) == M) { if constexpr (R < NPA) { pa[R] += delta; PIN(pa[R]); } else { qa[R - NPA] += delta; PIN(qa[R - NPA]); } }
      adv_at<M, R + 1>(delta);
    }
  }
  template <int KS, bool RD, int M> DI void grp(bf16x8 (&cp)[NI], bf16x8 (&cq)[NJ], bf16x8 (&np)[NI], bf16x8 (&nq)[NJ], int delta) {
    if constexpr (M < NM) {
      constexpr int mi = M / NJ, mj = (mi & 1) ? NJ - 1 - M % NJ : M % NJ;
      if constexpr (mi >= SWAPI) acc[mi][mj] = mfma16(cq[mj], ROLLP ? p0[mi] : cp[mi], acc[mi][mj]);
      else acc[mi][mj] = mfma16(ROLLP ? p0[mi] : cp[mi], cq[mj], acc[mi][mj]);
      SBAR();
      if constexpr (RD && KS == 0 && M == (ROLLP ? 2 : 0)) { sync_lds(); SBAR(); }
      if constexpr (RD && ROLLP) {
        static_assert(!ROLLP || (NJ == 4 && PIC), "rolling form");
        constexpr int qj = KS == 0 ? (M == 2 ? 0 : (M == 5 ? 1 : (M == 6 ? 2 : (M == 9 ? 3 : -1)))) : (M < 3 ? M : (M == 5 ? 3 : -1));
        if constexpr (qj >= 0) nq[qj] = lds_rd128(qa[KS] + qj * 2048);
        if constexpr (M % NJ == NJ - 1) {
          if constexpr (mi < NI - 1) tlo = lds_rdtr(pa[mi] + KS * 4096); else p0[mi] = cat8(lds_rdtr(pa[mi] + KS * 4096), lds_rdtr(pa[mi] + KS * 4096 + 512)); }
        if constexpr (M % NJ == 0 && M > 0) p0[mi - 1] = cat8(tlo, lds_rdtr(pa[mi - 1] + KS * 4096 + 512));
      } else if constexpr (RD && M < RG) {
        constexpr int lo = (M * NIT + RG - 1) / RG, hi = M + 1 < RG ? ((M + 1) * NIT + RG - 1) / RG : NIT;
        static_assert(hi - lo <= 2, "reads per gap");
        if constexpr (lo < hi) rd<KS, lo>(np, nq);
        if constexpr (lo + 1 < hi) rd<KS, lo + 1>(np, nq);
      }
      if constexpr (RD && KS == 1) adv_at<M, 0>(delta);
      SBAR();
      grp<KS, RD, M + 1>(cp, cq, np, nq, delta);
    }
  }
  template <int NT> DI void run(int s0) {
#pragma unroll
    for (int i = 0; i < NI; ++i) {
#pragma unroll
      for (int j = 0; j < NJ; ++j) acc[i][j] = f32x4{};
      if constexpr (ROLLP) p0[i] = bf16x8{}; else p1[i] = bf16x8{};
    }
#pragma unroll
    for (int j = 0; j < NJ; ++j) q1[j] = bf16x8{};
    int slot = s0;
    grp<0, true, 0>(p1, q1, p0, q0, 0);
    for (int t = 1; t < NT; ++t) {
      const int d = slot == 3 ? -3 * SLOT : SLOT; slot = (slot + 1) & 3;
      grp<1, true, 0>(p0, q0, p1, q1, d);
      grp<0, true, 0>(p1, q1, p0, q0, 0);
    }
    grp<1, true, 0>(p0, q0, p1, q1, 0);
    grp<0, false, 0>(p1, q1, p0, q0, 0);
  }
};

template <bool SCALE> DI void prep64(const f32x4& t00, const f32x4& t10, const f32x4& t01, const f32x4& t11, float sc, unsigned stg, int lane, u32x4* r) {
  const int j = lane & 15, g = lane >> 4;
  auto put = [&](const f32x4& v, int ii, int jj) { const int row = 16 * jj + j;
    const u32x2 w = SCALE ? u32x2{cvtpk(v[0] * sc, v[1] * sc), cvtpk(v[2] * sc, v[3] * sc)} : u32x2{cvtpk(v[0], v[1]), cvtpk(v[2], v[3])};
    lds_wr64(stg + row * 64 + (((2 * ii + (g >> 1)) ^ ((row >> 1) & 3)) * 16) + (g & 1) * 8, w); };
  put(t00, 0, 0); put(t10, 1, 0); put(t01, 0, 1); put(t11, 1, 1);
#pragma unroll
  for (int m = 0; m < 2; ++m) { const int rr = 16 * m + (lane >> 2), c = lane & 3; r[m] = lds_rdu(stg + rr * 64 + ((c ^ ((rr >> 1) & 3)) * 16)); }
}
DI void commit64(const u32x4* r, int lane, rsrc_t rs, unsigned soff, unsigned ldb) {
#pragma unroll
  for (int m = 0; m < 2; ++m) st16(rs, (16 * m + (lane >> 2)) * ldb + (lane & 3) * 16, soff, r[m]);
}
DI void read128(unsigned stg, int lane, u32x4* r) {
#pragma unroll
  for (int m = 0; m < 4; ++m) { const int row = 8 * m + (lane >> 3), c = lane & 7; r[m] = lds_rdu(stg + row * 128 + ((c ^ (row & 7)) * 16)); }
}
DI void commit128(const u32x4* r, int lane, rsrc_t rs, unsigned soff, unsigned ldb) {
#pragma unroll
  for (int m = 0; m < 4; ++m) st16(rs, (8 * m + (lane >> 3)) * ldb + (lane & 7) * 16, soff, r[m]);
}
template <int NJ> DI void prep128(const f32x4 (&acc)[4][NJ], int j32, unsigned sgn, unsigned stg, int lane, u32x4* r) {
  const int j = lane & 15, g = lane >> 4;
#pragma unroll
  for (int jj = 0; jj < 2; ++jj)
#pragma unroll
    for (int ii = 0; ii < 4; ++ii) { const f32x4& v = acc[ii][2 * j32 + jj]; const int row = 16 * jj + j;
      lds_wr64(stg + row * 128 + (((2 * ii + (g >> 1)) ^ (row & 7)) * 16) + (g & 1) * 8, u32x2{cvtpk(v[0], v[1]) ^ sgn, cvtpk(v[2], v[3]) ^ sgn}); }
  read128(stg, lane, r);
}

DI int iperm(int r) { const int b = r & 3, h = (r >> 2) & 1, t = r >> 3; return b + 4 * (t & 1) + 8 * h + 16 * (t >> 1); }
DI unsigned k_voff(int lane) { return (unsigned)((lane >> 2) * 64 + (((lane & 3) ^ ((lane >> 4) & 3)) * 16)); }
DI unsigned v_voff(int lane) { const int c = lane & 7; return (unsigned)((lane >> 3) * 4096 + ((c & 4) | ((c & 3) ^ ((lane >> 4) & 3))) * 16); }
DI unsigned k_raddr(int lane, int s) { const int r = lane & 31, h = lane >> 5; return (unsigned)(r * 64 + (((2 * s + h) ^ ((r >> 2) & 3)) * 16)); }
DI unsigned v_raddr(int lane, int s) { const int d = iperm(lane & 31), h = lane >> 5; return (unsigned)(d * 128 + (((2 * s + h) ^ ((d >> 1) & 3)) * 16)); }
DI float swap32_max(float m) { auto rr = __builtin_amdgcn_permlane32_swap(__float_as_uint(m), __float_as_uint(m), false, false); return __builtin_fmaxf(__uint_as_float(rr[0]), __uint_as_float(rr[1])); }
DI float swap32_sum(float m) { auto rr = __builtin_amdgcn_permlane32_swap(__float_as_uint(m), __float_as_uint(m), false, false); return __uint_as_float(rr[0]) + __uint_as_float(rr[1]); }

struct AttnWave {
  f32x16 S[2], NM[2], O[2]; f32x4 L[2];
  u32x4 P[2][2];
  bf16x8 q[2][2], kf[2], vf[2];
  bf16x8 asel; int sidx;
  unsigned ka0, ka1, va0, va1;
  template <int X> DI void exp4(int i) { S[X][i] = ex2(S[X][i]); S[X][i + 1] = ex2(S[X][i + 1]); S[X][i + 2] = ex2(S[X][i + 2]); S[X][i + 3] = ex2(S[X][i + 3]); PIN(S[X]); }
  template <int X> DI void cvt2(int i) { P[X][i >> 3][(i >> 1) & 3] = cvtpk(S[X][i], S[X][i + 1]); P[X][i >> 3][((i >> 1) & 3) + 1] = cvtpk(S[X][i + 2], S[X][i + 3]); PIN(P[X][i >> 3]); }
  template <int X, int RD, bool AFTER_RD> DI void slot(int delta = 0) {
    constexpr int Y = X ^ 1, B = RD - 1;
    if constexpr (AFTER_RD) __builtin_amdgcn_s_waitcnt(0xC27F);
    S[Y] = mfma32(kf[0], q[Y][0], NM[Y]); SBAR();
    if constexpr (RD == 1) { ka0 += delta; ka1 += delta; va0 += delta; va1 += delta; PIN(ka0); PIN(ka1); PIN(va0); PIN(va1); sync_lds(); SBAR(); }
    exp4<X>(0); SBAR();
    S[Y] = mfma32(kf[1], q[Y][1], S[Y]); SBAR();
    if constexpr (RD) { kf[0] = lds_rd128(ka0 + B * 2048); SBAR(); }
    exp4<X>(4); cvt2<X>(0); SBAR();
    if constexpr (AFTER_RD) __builtin_amdgcn_s_waitcnt(0xC07F);
    O[Y] = mfma32(vf[0], as_bf(P[Y][0]), O[Y]); SBAR();
    if constexpr (RD) { kf[1] = lds_rd128(ka1 + B * 2048); SBAR(); }
    exp4<X>(8); cvt2<X>(4); SBAR();
    O[Y] = mfma32(vf[1], as_bf(P[Y][1]), O[Y]); SBAR();
    if constexpr (RD) { vf[0] = lds_rd128(va0 + B * 64); SBAR(); }
    exp4<X>(12); cvt2<X>(8); SBAR();
    L[Y] = __builtin_amdgcn_smfmac_f32_16x16x64_bf16(asel, cat16(P[Y][0], P[Y][1]), L[Y], sidx, 0, 0); SBAR();
    if constexpr (RD) { vf[1] = lds_rd128(va1 + B * 64); SBAR(); }
    cvt2<X>(12); SBAR();
  }
  DI void run(int sl) {
    slot<0, 1, false>(0); slot<1, 0, true>();
    for (int t = 1; t < 32; ++t) {
      const int d = sl == 3 ? -3 * SLOT : SLOT; sl = (sl + 1) & 3;
      slot<0, 2, false>(); slot<1, 0, true>(); slot<0, 1, false>(d); slot<1, 0, true>();
    }
    slot<0, 2, false>(); slot<1, 0, true>();
    O[1] = mfma32(vf[0], as_bf(P[1][0]), O[1]);
    O[1] = mfma32(vf[1], as_bf(P[1][1]), O[1]);
    L[1] = __builtin_amdgcn_smfmac_f32_16x16x64_bf16(asel, cat16(P[1][0], P[1][1]), L[1], sidx, 0, 0);
  }
};
DI void attn_exact(rsrc_t rk, rsrc_t rv, const bf16x8 (&q)[2], int lane, f32x16& O, float& l) {
  const int r = lane & 31, h = lane >> 5;
  float m = -INFINITY; l = 0.f; O = f32x16{};
  const unsigned ko = r * 64 + h * 16, vo = iperm(r) * 4096 + h * 16;
  for (int j = 0; j < 64; ++j) {
    f32x16 s = mfma32(as_bf(ld16s(rk, ko, j * 2048)), q[0], f32x16{});
    s = mfma32(as_bf(ld16s(rk, ko + 32, j * 2048)), q[1], s);
    float mx = s[0];
#pragma unroll
    for (int i = 1; i < 16; ++i) mx = __builtin_fmaxf(mx, s[i]);
    const float mn = __builtin_fmaxf(m, swap32_max(mx));
    const float f = ex2(m - mn); m = mn;
    float ps = 0.f; u32x4 p0, p1;
#pragma unroll
    for (int i = 0; i < 16; ++i) s[i] = ex2(s[i] - mn);
#pragma unroll
    for (int i = 0; i < 4; ++i) { p0[i] = cvtpk(s[2 * i], s[2 * i + 1]); p1[i] = cvtpk(s[8 + 2 * i], s[9 + 2 * i]); }
#pragma unroll
    for (int i = 0; i < 4; ++i) { ps += __uint_as_float(p0[i] << 16) + __uint_as_float(p0[i] & 0xffff0000u) + __uint_as_float(p1[i] << 16) + __uint_as_float(p1[i] & 0xffff0000u); }
    l = l * f + swap32_sum(ps);
    O = O * f;
    O = mfma32(as_bf(ld16s(rv, vo, j * 64)), as_bf(p0), O);
    O = mfma32(as_bf(ld16s(rv, vo + 32, j * 64)), as_bf(p1), O);
  }
}

template <int IMM, bool SC1> DI void piece(unsigned sb, unsigned voff, rsrc_t rs, unsigned soff) {
  if constexpr (SC1) asm volatile("s_add_u32 m0, %0, %1\n\ts_nop 0\n\tbuffer_load_dwordx4 %2, %3, %4 offen sc1 lds" ::"s"(sb), "n"(IMM), "v"(voff), "s"(rs), "s"(soff) : "memory", "scc");
  else asm volatile("s_add_u32 m0, %0, %1\n\ts_nop 0\n\tbuffer_load_dwordx4 %2, %3, %4 offen lds" ::"s"(sb), "n"(IMM), "v"(voff), "s"(rs), "s"(soff) : "memory", "scc");
}
template <bool PS> struct KKStream {
  rsrc_t rp, rq; unsigned vp[4], vq[4];
  DI void init(int lane, int w, const void* p, unsigned pbytes, unsigned prow0, const void* q, unsigned qbytes, unsigned qrow0, unsigned ld) {
    rp = make_rsrc(p, pbytes); rq = make_rsrc(q, qbytes);
#pragma unroll
    for (int g = 0; g < 4; ++g) { const unsigned kv = kc_voff(lane, g & 1, ld); vp[g] = kv + (prow0 + (4 * w + g) * 8) * ld; vq[g] = kv + (qrow0 + (4 * w + g) * 8) * ld; }
  }
  DI void issue(unsigned slot0, int w, int t) {
    const unsigned sb = slot0 + w * 4096, so = t * 128;
    piece<16384, false>(sb, vq[0], rq, so); piece<17408, false>(sb, vq[1], rq, so); piece<18432, false>(sb, vq[2], rq, so); piece<19456, false>(sb, vq[3], rq, so);
    piece<0, PS>(sb, vp[0], rp, so); piece<1024, PS>(sb, vp[1], rp, so); piece<2048, PS>(sb, vp[2], rp, so); piece<3072, PS>(sb, vp[3], rp, so);
  }
};
struct T1Stream {
  rsrc_t rw, rx; unsigned vw[6], vx[4];
  DI void init(const Args& a, int lane, int w, unsigned s0, unsigned ct) {
    rw = make_rsrc(a.wqkv, 1024 * 3072 * 2); rx = make_rsrc(a.Ax, 2048 * 1024 * 2);
#pragma unroll
    for (int p = 0; p < 3; ++p)
#pragma unroll
      for (int g = 0; g < 2; ++g) vw[2 * p + g] = ic_voff(lane, g, 6144) + (2 * w + g) * 8 * 6144u + p * 2048u + ct * 128u;
#pragma unroll
    for (int g = 0; g < 4; ++g) vx[g] = kc_voff(lane, g & 1, 2048) + (s0 + (4 * w + g) * 8) * 2048u;
  }
  DI void issue(unsigned slot0, int w, int t) {
    const unsigned sw = slot0 + w * 2048, sx = slot0 + 24576 + w * 4096, ow = t * (64u * 6144u), ox = t * 128;
    piece<0, false>(sw, vw[0], rw, ow); piece<1024, false>(sw, vw[1], rw, ow); piece<8192, false>(sw, vw[2], rw, ow);
    piece<9216, false>(sw, vw[3], rw, ow); piece<16384, false>(sw, vw[4], rw, ow); piece<17408, false>(sw, vw[5], rw, ow);
    piece<0, false>(sx, vx[0], rx, ox); piece<1024, false>(sx, vx[1], rx, ox); piece<2048, false>(sx, vx[2], rx, ox); piece<3072, false>(sx, vx[3], rx, ox);
  }
};
struct KVStream {
  rsrc_t rk, rv; unsigned vk, vv;
  DI void init(const Args& a, int lane, int w, int head) {
    rk = make_rsrc(a.Q + (size_t)(32 + head) * 65536, 65536 * 2); rv = make_rsrc(a.Q + (size_t)(64 + head) * 65536, 65536 * 2);
    vk = k_voff(lane) + w * 1024; vv = v_voff(lane) + w * 8 * 4096;
  }
  DI void issue(unsigned slot0, int w, int t) { const unsigned sb = slot0 + w * 1024; piece<32768, true>(sb, vk, rk, t * 4096 + 2048); piece<36864, true>(sb, vv, rv, t * 128); }
  DI void issue_first(unsigned slot0, int w) { const unsigned sb = slot0 + w * 1024; piece<32768, true>(sb, vk, rk, 0); piece<36864, true>(sb, vk, rk, 4096); }
};
struct T5Stream {
  rsrc_t rw, ro; unsigned vw[4], vo[2];
  DI void init(const Args& a, int lane, int w, unsigned n0, unsigned s5) {
    rw = make_rsrc(a.wo, 1024 * 1024 * 2); ro = make_rsrc(a.O, 2048 * 1024 * 2);
#pragma unroll
    for (int p = 0; p < 2; ++p)
#pragma unroll
      for (int g = 0; g < 2; ++g) vw[2 * p + g] = ic_voff(lane, g, 2048) + (2 * w + g) * 8 * 2048u + (n0 + p * 64) * 2u;
#pragma unroll
    for (int g = 0; g < 2; ++g) vo[g] = kc_voff(lane, g, 2048) + (s5 + (2 * w + g) * 8) * 2048u;
  }
  DI void issue(unsigned slot0, int w, int t) {
    const unsigned sb = slot0 + w * 2048, ow = t * (64u * 2048u), oo = t * 128;
    piece<0, false>(sb, vw[0], rw, ow); piece<1024, false>(sb, vw[1], rw, ow); piece<8192, false>(sb, vw[2], rw, ow); piece<9216, false>(sb, vw[3], rw, ow);
    piece<16384, true>(sb, vo[0], ro, oo); piece<17408, true>(sb, vo[1], ro, oo);
  }
};
constexpr unsigned OOB = 0x7ff00000u;
DI void l2warm(unsigned voff, rsrc_t rs, unsigned soff) { asm volatile("buffer_load_dword v167, %0, %1, %2 offen sc1" ::"v"(voff), "s"(rs), "s"(soff) : "memory", "v167"); }
struct Warm {
  rsrc_t rs; unsigned v, dv; u64 mask;
  DI void fire() { l2warm(v, rs, 0u); v += dv; }
  DI void step(int s) { const bool act = s < 64 && ((mask >> s) & 1); l2warm(v, rs, act ? 0u : OOB); if (act) v += dv; }
};
constexpr u64 bit_(int s) { return 1ull << s; }
constexpr __host__ __device__ bool in_(int s, int a, int n) { return s >= a && s < a + n; }
constexpr __host__ __device__ int npieces(int s) { return (s < 16 ? 10 : 0) + ((in_(s, S_T2, 16) || in_(s, S_T4, 32)) ? 8 : 0) + (in_(s, S_A, 32) ? 2 : 0) + (in_(s, S_T5, 16) ? 6 : 0); }
constexpr __host__ __device__ int npf(int s) { return s == S_WO1 ? 2 : 1; }
constexpr __host__ __device__ int nwait(int s) { return npieces(s + 1) + npieces(s + 2) + (s == S_A - 1 ? 0 : npf(s - 3)) + npf(s - 2) + npf(s - 1); }
constexpr __host__ __device__ int run_end(int s0, int s1) { int lo = nwait(s0), hi = lo, e = s0 + 1;
  for (; e < s1; ++e) { const int n = nwait(e), l = n < lo ? n : lo, h = n > hi ? n : hi; if (h - l > 2) break; lo = l; hi = h; }
  return e; }
constexpr __host__ __device__ int run_wait(int s0, int e) { int lo = nwait(s0); for (int s = s0; s < e; ++s) if (nwait(s) < lo) lo = nwait(s); return lo; }
template <int S0, int S1, class F> DI void for_steps(F&& f) {
  if constexpr (S0 < S1) {
    constexpr int E = run_end(S0, S1), W = run_wait(S0, E);
#pragma nounroll
    for (int s = S0; s < E; ++s) { wait_vm<W>(); barrier(); f(s); }
    for_steps<E, S1>(f);
  }
}
DI void role_dma(const Args& a, const Ids& id) {
  const int lane = id.lane, w = id.wid - 8, xcd = id.xcd, idx = id.idx;
  const int rl = idx & 7, cl = idx >> 3, head = xcd * 4 + cl, half = xcd & 1, tb = idx & 3, eb = idx >> 2;
  const unsigned s0 = ((xcd & 1) * 8 + rl) * 128, ct = (xcd >> 1) * 4 + cl, eg = ct * 128;
  const unsigned t0 = (xcd >> 1) * 512 + tb * 128, e0 = eb * 128;
  const unsigned n0 = ((xcd & 1) * 4 + tb) * 128, s5 = ((xcd >> 1) * 8 + eb) * 64;
  const int ph1 = (rl + cl) & 3, ph4 = (tb + eb) & 1;
  auto slot_of = [&](int s) { return id.lds0 + (s & 3) * SLOT; };
  T1Stream t1; t1.init(a, lane, w, s0, ct);
  t1.issue(slot_of(0), w, 0);
  Warm f;
  const unsigned vx = (s0 + 32 * cl + (lane & 31)) * 2048u + ((lane >> 5) + 2 * w) * 128u;
  { const int wl = (w & 1) * 64 + lane;
    f.rs = w < 2 ? make_rsrc(a.Ax, 2048 * 1024 * 2) : make_rsrc(a.wqkv, 1024 * 3072 * 2);
    f.v = w < 2 ? vx : (wl < 96 ? (32 * rl + wl / 3) * 6144u + (wl % 3) * 2048u + ct * 128u : OOB);
    f.dv = w < 2 ? 512u : (wl < 96 ? 256u * 6144u : 0u); }
  f.fire();
  wait_vm<1>();
  barrier();
  t1.issue(slot_of(1), w, 1); t1.issue(slot_of(2), w, 2); t1.issue(slot_of(3), w, 3);
  f.fire();
  f.mask = (bit_(0) | bit_(4)) << ph1; f.step(0);
  l2warm(OOB, f.rs, OOB);
  for_steps<1, 8>([&](int s) { t1.issue(slot_of(s + 3), w, s + 3); f.step(s); });
  const void* wd = eg >= 1024 ? a.Wi : a.Wr;
  f.rs = w < 2 ? make_rsrc(a.Bx, 2048 * 1024 * 2) : make_rsrc(wd, 1024 * 1024 * 2);
  f.v = w < 2 ? vx : (w == 2 ? ((eg & 1023) + 16 * rl + (lane & 15)) * 2048u + (lane >> 4) * 128u : OOB);
  f.dv = w < 3 ? 512u : 0u;
  f.mask = (bit_(13) | bit_(14) | bit_(S_T2 + 1) | bit_(S_T2 + 5)) << (ph1 & 1);
  for_steps<8, 13>([&](int s) { t1.issue(slot_of(s + 3), w, s + 3); f.step(s); });
  KKStream<false> g2; g2.init(lane, w, a.Bx, 2048 * 1024 * 2, s0, wd, 1024 * 1024 * 2, eg & 1023, 2048);
  for_steps<13, S_T2 - 3>([&](int s) { f.step(s); });
  for_steps<S_T2 - 3, S_A - 3>([&](int s) { g2.issue(slot_of(s + 3), w, s + 3 - S_T2); f.step(s); });
  KVStream kv; kv.init(a, lane, w, head);
  if (w == 0) { f.rs = make_rsrc(half ? a.Ci : a.Cr, 2048 * 2048 * 2); f.v = (t0 + 16 * eb + (lane & 15)) * 4096u + (lane >> 4) * 128u; f.dv = 512;
    f.mask = (bit_(S_T2 + 11) | bit_(S_T2 + 13) | bit_(S_T4 - 3) | bit_(S_T4 + 1) | bit_(S_T4 + 4) | bit_(S_T4 + 7) | bit_(S_T4 + 10) | bit_(S_T4 + 13)) << ph4; }
  else if (w < 3) { f.rs = make_rsrc(a.ABt + (size_t)half * 1024 * 2048, 1024 * 2048 * 2);
    f.v = (e0 + 32 * tb + (lane & 31)) * 4096u + ((lane >> 5) + 2 * (w - 1)) * 128u + 512; f.dv = 512;
    f.mask = bit_(S_T4 - 3) | ((bit_(S_T4 - 1) | bit_(S_T4 + 2) | bit_(S_T4 + 5) | bit_(S_T4 + 8) | bit_(S_T4 + 11) | bit_(S_T4 + 14)) << ph4); }
  else { f.rs = make_rsrc(a.Q + (size_t)(32 + head) * 65536, 33 * 65536 * 2);
    f.v = lane < 16 ? (rl * 16 + lane) * 128u + 16384u : (lane < 32 ? 32u * 65536 * 2 + (rl * 4 + ((lane >> 2) & 3)) * 4096u + (lane & 3) * 128u + 512u : OOB);
    f.dv = lane < 16 ? 16384u : (lane < 32 ? 512u : 0u);
    f.mask = bit_(S_A - 3) | ((bit_(S_A - 1) | bit_(S_A + 2) | bit_(S_A + 5) | bit_(S_A + 12) | bit_(S_A + 16) | bit_(S_A + 20)) << (idx & 1)); }
  for_steps<S_A - 3, S_A - 2>([&](int s) { kv.issue_first(slot_of(S_A - 1), w); g2.issue(slot_of(s + 3), w, s + 3 - S_T2); kv.issue(slot_of(s + 3), w, s + 3 - S_A); f.step(s); });
  for_steps<S_A - 2, S_T2 + 13>([&](int s) { g2.issue(slot_of(s + 3), w, s + 3 - S_T2); kv.issue(slot_of(s + 3), w, s + 3 - S_A); f.step(s); });
  for_steps<S_T2 + 13, S_T4 - 3>([&](int s) { kv.issue(slot_of(s + 3), w, s + 3 - S_A); f.step(s); });
  KKStream<true> g4; g4.init(lane, w, a.ABt + (size_t)half * 1024 * 2048, 1024 * 2048 * 2, e0, half ? a.Ci : a.Cr, 2048 * 2048 * 2, t0, 4096);
  Warm fw; fw.rs = make_rsrc(a.wo, 1024 * 1024 * 2); fw.v = (128 * eb + (w & 1) * 64 + lane) * 2048u + n0 * 2 + (w >> 1) * 128u; fw.dv = 0;
  for_steps<S_T4 - 3, S_A + 29>([&](int s) { g4.issue(slot_of(s + 3), w, s + 3 - S_T4); kv.issue(slot_of(s + 3), w, s + 3 - S_A); f.step(s); if (s == S_WO1) fw.fire(); });
  for_steps<S_A + 29, S_T5 - 3>([&](int s) { g4.issue(slot_of(s + 3), w, s + 3 - S_T4); if (s == S_WO2) fw.fire(); else f.step(s); });
  T5Stream t5; t5.init(a, lane, w, n0, s5);
  for_steps<S_T5 - 3, S_END - 3>([&](int s) { t5.issue(slot_of(s + 3), w, s + 3 - S_T5); f.step(s); });
  for_steps<S_END - 3, S_END>([&](int s) { f.step(s); });
}

DI void role_g0(const Args& a, const Ids& id) {
  const int wid = id.wid, wi = wid & 1, wj = wid >> 1;
  u64 ep = load_epoch(a);
  {
    const int lane = id.lane, rl = id.idx & 7, cl = id.idx >> 3, rb = (id.xcd & 1) * 8 + rl, ct = (id.xcd >> 1) * 4 + cl;
    const int s0 = rb * 128 + 64 * wj, head = 2 * ct + wi;
    Cons<6, 4, true, 4, true> c;
#pragma unroll
    for (int i = 0; i < 6; ++i) c.pa[i] = id.lds0 + ic_raddr(lane, 4 * (i >> 1) + 2 * wi + (i & 1));
#pragma unroll
    for (int k = 0; k < 2; ++k) c.qa[k] = id.lds0 + 24576 + kc_raddr(lane, 64 * wj, k);
    c.run<16>(0);
    barrier();
    ep = uniform64(ep);
    const int ln = fresh_lane();
    const unsigned stg = id.lds0 + wj * SLOT + 32768 + wi * 4096;
    const rsrc_t rq = make_rsrc(a.Q, 3u * 32 * 65536 * 2);
    const unsigned oq = (unsigned)(head * 65536 + s0 * 32) * 2u, ok = oq + 32 * 65536 * 2;
    u32x4 r[4];
    prep64<true>(c.acc[0][0], c.acc[1][0], c.acc[0][1], c.acc[1][1], QSCALE, stg, ln, r);
    prep64<true>(c.acc[0][2], c.acc[1][2], c.acc[0][3], c.acc[1][3], QSCALE, stg + 2048, ln, r + 2);
    barrier();
    commit64(r, ln, rq, oq, 64); commit64(r + 2, ln, rq, oq + 2048, 64);
    prep64<false>(c.acc[2][0], c.acc[3][0], c.acc[2][1], c.acc[3][1], 1.0f, stg, ln, r);
    prep64<false>(c.acc[2][2], c.acc[3][2], c.acc[2][3], c.acc[3][3], 1.0f, stg + 2048, ln, r + 2);
    barrier();
    commit64(r, ln, rq, ok, 64); commit64(r + 2, ln, rq, ok + 2048, 64);
    {
      const int dl = ln & 15, g = ln >> 4, pos = (g & 1) * 2 + (g >> 1);
#pragma unroll
      for (int db = 0; db < 2; ++db)
#pragma unroll
        for (int jj = 0; jj < 4; ++jj) { const f32x4& v = c.acc[4 + db][jj]; const int dd = 16 * db + dl;
          lds_wr64(stg + dd * 128 + (((2 * jj + (pos >> 1)) ^ (dd & 7)) * 16) + (pos & 1) * 8, u32x2{cvtpk(v[0], v[1]), cvtpk(v[2], v[3])}); }
      read128(stg, ln, r);
    }
    barrier();
    commit128(r, ln, rq, (unsigned)((64 + head) * 65536 + s0) * 2u, 4096);
    barrier();
    signal(a.flg, F_QKV + ct * 64 + rb * 4 + wid, ep, ln);
    idle(S_A - 3 - 21);
    if (wid == 0) await(a.flg, F_QKV + ((id.xcd * 4 + cl) >> 1) * 64, 64, ep, ln);
    barrier();
  }
  {
    const int lane = fresh_lane(), r = lane & 31, h = lane >> 5;
    const int head = id.xcd * 4 + (id.idx >> 3), qb = id.idx & 7, q0 = qb * 256 + wid * 64;
    const rsrc_t rq = make_rsrc(a.Q + (size_t)head * 65536, 65536 * 2);
    AttnWave w;
#pragma unroll
    for (int b = 0; b < 2; ++b)
#pragma unroll
      for (int s = 0; s < 2; ++s) w.q[b][s] = as_bf(ld16s(rq, (q0 + 32 * b + r) * 64 + 32 * s + 16 * h, 0));
    idle(2);
    const unsigned kp = id.lds0 + ((S_A - 1) & 3) * SLOT + 32768;
    bf16x8 k0[4][2];
#pragma unroll
    for (int j = 0; j < 4; ++j) { k0[j][0] = lds_rd128(kp + k_raddr(lane, 0) + j * 2048); k0[j][1] = lds_rd128(kp + k_raddr(lane, 1) + j * 2048); }
#pragma unroll
    for (int b = 0; b < 2; ++b) {
      float mx = -INFINITY;
#pragma unroll
      for (int j = 3; j >= 0; --j) {
        f32x16 s = mfma32(k0[j][0], w.q[b][0], f32x16{}); s = mfma32(k0[j][1], w.q[b][1], s);
#pragma unroll
        for (int i = 0; i < 16; ++i) mx = __builtin_fmaxf(mx, s[i]);
        if (j == 0 && b == 0) w.S[0] = s;
      }
      const float nm = -(swap32_max(mx) + MARGIN);
#pragma unroll
      for (int i = 0; i < 16; ++i) w.NM[b][i] = nm;
      if (b == 0) w.S[0] = w.S[0] + nm;
      w.O[b] = f32x16{}; w.L[b] = f32x4{};
    }
    w.kf[0] = k0[0][0]; w.kf[1] = k0[0][1];
    w.S[1] = f32x16{};
    w.P[1][0] = u32x4{}; w.P[1][1] = u32x4{}; w.P[0][0] = u32x4{}; w.P[0][1] = u32x4{};
    w.vf[0] = bf16x8{}; w.vf[1] = bf16x8{};
    {
      const int m = lane & 15; const __bf16 one = (__bf16)1.0f, z = (__bf16)0.0f; const bool lo = (m == 0 || m == 4), hi = (m == 8 || m == 12);
      w.asel = bf16x8{lo ? one : z, lo ? one : z, lo ? one : z, lo ? one : z, hi ? one : z, hi ? one : z, hi ? one : z, hi ? one : z};
      w.sidx = (m == 4 || m == 12) ? 0xEEEE : 0x4444; }
    constexpr unsigned so = (S_A & 3) * SLOT;
    w.ka0 = id.lds0 + so + 32768 + k_raddr(lane, 0); w.ka1 = id.lds0 + so + 32768 + k_raddr(lane, 1);
    w.va0 = id.lds0 + so + 36864 + v_raddr(lane, 0); w.va1 = id.lds0 + so + 36864 + v_raddr(lane, 1);
    w.run(S_A & 3);
    barrier();
    const int ln = fresh_lane(), r2 = ln & 31, h2 = ln >> 5;
    const unsigned stg = id.lds0 + ((S_A + 32 + wj) & 3) * SLOT + 32768 + wi * 4096;
    u32x4 ro[2];
#pragma unroll
    for (int b = 0; b < 2; ++b) {
      const int src = (r2 & 15) + (r2 >> 4) * 32;
      float l = __shfl(w.L[b][0], src) + __shfl(w.L[b][0], src + 16);
      float amax = 0.f;
#pragma unroll
      for (int i = 0; i < 16; ++i) amax = __builtin_fmaxf(amax, __builtin_fabsf(w.O[b][i]));
      bool nan = false;
#pragma unroll
      for (int i = 0; i < 16; ++i) nan |= !(w.O[b][i] == w.O[b][i]);
      const bool bad = !(l < 0x1p126f) || !(amax < 0x1p126f) || nan || (amax < 0x1p-115f);
      if (__any(bad)) attn_exact(make_rsrc(a.Q + (size_t)(32 + head) * 65536, 65536 * 2), make_rsrc(a.Q + (size_t)(64 + head) * 65536, 65536 * 2), w.q[b], ln, w.O[b], l);
      const float inv = 1.0f / l;
      const f32x16 v = w.O[b] * inv;
      lds_wr128(stg + b * 2048 + r2 * 64 + ((h2 ^ ((r2 >> 1) & 3)) * 16), u32x4{cvtpk(v[0], v[1]), cvtpk(v[2], v[3]), cvtpk(v[4], v[5]), cvtpk(v[6], v[7])});
      lds_wr128(stg + b * 2048 + r2 * 64 + (((2 + h2) ^ ((r2 >> 1) & 3)) * 16), u32x4{cvtpk(v[8], v[9]), cvtpk(v[10], v[11]), cvtpk(v[12], v[13]), cvtpk(v[14], v[15])});
#pragma unroll
      for (int m = 0; m < 2; ++m) { const int row = 32 * b + 16 * m + (ln >> 2), c = ln & 3; ro[m] = lds_rdu(stg + row * 64 + ((c ^ ((row >> 1) & 3)) * 16)); }
      barrier();
      commit64(ro, ln, make_rsrc(a.O, 2048 * 1024 * 2), (unsigned)((q0 + 32 * b) * 1024 + head * 32) * 2u, 2048);
    }
    barrier();
    signal(a.flg, F_O + qb * 128 + head * 4 + wid, ep, ln);
    idle(S_T5 - 3 - (S_A + 36));
    if (wid == 0) {
      await(a.flg, F_O + ((id.xcd >> 1) * 2 + (id.idx >> 4)) * 128, 128, ep, ln);
      if (blockIdx.x == 0 && ln == 0) __hip_atomic_store(a.flg + F_EPOCH, ep, __ATOMIC_RELAXED, __HIP_MEMORY_SCOPE_AGENT);
    }
    idle(3);
  }
  {
    const int lane = fresh_lane();
    const int n0 = ((id.xcd & 1) * 4 + (id.idx & 3)) * 128, s5 = ((id.xcd >> 1) * 8 + (id.idx >> 2)) * 64;
    Cons<4, 2, true> c;
    constexpr unsigned so = (S_T5 & 3) * SLOT;
#pragma unroll
    for (int i = 0; i < 4; ++i) c.pa[i] = id.lds0 + so + ic_raddr(lane, 4 * wi + i);
#pragma unroll
    for (int k = 0; k < 2; ++k) c.qa[k] = id.lds0 + so + 16384 + kc_raddr(lane, 32 * wj, k);
    c.run<16>(S_T5 & 3);
    const int ln = fresh_lane();
    u32x4 r[4];
    prep128(c.acc, 0, 0u, id.lds0 + (2 + wj) * SLOT + 32768 + wi * 4096, ln, r);
    commit128(r, ln, make_rsrc(a.out, 2048 * 2048 * 2), (unsigned)((s5 + 32 * wj) * 2048 + n0 + 64 * wi) * 2u, 4096);
  }
}

DI void role_g1(const Args& a, const Ids& id) {
  const int wi = id.wid & 1, wj = (id.wid >> 1) & 1, xcd = id.xcd, idx = id.idx;
  u64 ep = load_epoch(a);
  {
    const int lane = id.lane, rb = (xcd & 1) * 8 + (idx & 7), ct = (xcd >> 1) * 4 + (idx >> 3);
    const int s0 = rb * 128, eg = ct * 128;
    Cons<4, 4, false> c;
    constexpr unsigned so = (S_T2 & 3) * SLOT;
#pragma unroll
    for (int k = 0; k < 2; ++k) { c.pa[k] = id.lds0 + so + kc_raddr(lane, 64 * wi, k); c.qa[k] = id.lds0 + so + 16384 + kc_raddr(lane, 64 * wj, k); }
    idle(8);
    ep = uniform64(ep);
    idle(S_T2 - 8);
    c.run<16>(S_T2 & 3);
    barrier();
    const int ln = fresh_lane();
    const unsigned stg = id.lds0 + so + (id.wid & 3) * 4096;
    const unsigned sgn = eg >= 1024 ? 0x80008000u : 0u;
    const rsrc_t rab = make_rsrc(a.ABt, 2048u * 2048 * 2);
    u32x4 r[4];
    prep128(c.acc, 0, sgn, stg, ln, r);
    barrier();
    commit128(r, ln, rab, (unsigned)((eg + 64 * wj) * 2048 + s0 + 64 * wi) * 2u, 4096);
    prep128(c.acc, 1, sgn, stg, ln, r);
    barrier();
    commit128(r, ln, rab, (unsigned)((eg + 64 * wj + 32) * 2048 + s0 + 64 * wi) * 2u, 4096);
    barrier();
    signal(a.flg, F_AB + ct * 64 + rb * 4 + (id.wid & 3), ep, ln);
    idle(S_T4 - 3 - (S_T2 + 20));
    if (id.wid == 4) await(a.flg, F_AB + ((xcd & 1) * 8 + (idx >> 2)) * 64, 64, ep, ln);
    idle(3);
  }
  {
    const int lane = fresh_lane(), half = xcd & 1, t0 = (xcd >> 1) * 512 + (idx & 3) * 128, e0 = (idx >> 2) * 128;
    Cons<4, 4, false> c;
    constexpr unsigned so = (S_T4 & 3) * SLOT;
#pragma unroll
    for (int k = 0; k < 2; ++k) { c.pa[k] = id.lds0 + so + kc_raddr(lane, 64 * wi, k); c.qa[k] = id.lds0 + so + 16384 + kc_raddr(lane, 64 * wj, k); }
    c.run<32>(S_T4 & 3);
    barrier();
    const int ln = fresh_lane();
    if (wj != half) {
      const rsrc_t rx = make_rsrc(a.X + (size_t)(blockIdx.x * 2 + wi) * 4096, 8192);
#pragma unroll
      for (int m = 0; m < 8; ++m) { const f32x4 &u = c.acc[m >> 1][(2 * m) & 3], &v = c.acc[m >> 1][(2 * m + 1) & 3];
        st16(rx, ln * 16, m * 1024, u32x4{cvtpk(u[0], u[1]), cvtpk(u[2], u[3]), cvtpk(v[0], v[1]), cvtpk(v[2], v[3])}); }
      idle(3);
      signal(a.flg, F_P + (blockIdx.x ^ 1) * 2 + wi, ep, ln);
      return;
    }
    idle(9);
    await(a.flg, F_P + blockIdx.x * 2 + wi, 1, ep, ln);
    const rsrc_t rx = make_rsrc(a.X + (size_t)((blockIdx.x ^ 1) * 2 + wi) * 4096, 8192);
    u32x4 y[8];
#pragma unroll
    for (int m = 0; m < 8; ++m) y[m] = ld16s(rx, ln * 16, m * 1024);
    idle(6);
#pragma unroll
    for (int m = 0; m < 8; ++m)
#pragma unroll
      for (int e = 0; e < 2; ++e) { f32x4& u = c.acc[m >> 1][(2 * m + e) & 3];
        u[0] += __uint_as_float(y[m][2 * e] << 16); u[1] += __uint_as_float(y[m][2 * e] & 0xffff0000u);
        u[2] += __uint_as_float(y[m][2 * e + 1] << 16); u[3] += __uint_as_float(y[m][2 * e + 1] & 0xffff0000u); }
    const unsigned stg = id.lds0 + 32768 + wi * 4096;
    const rsrc_t ro = make_rsrc(a.out, 2048 * 2048 * 2);
#pragma unroll
    for (int p = 0; p < 2; ++p) {
      u32x4 r[4]; prep128(c.acc, p, 0u, stg, ln, r);
      commit128(r, ln, ro, (unsigned)((t0 + 64 * wj + 32 * p) * 2048 + 1024 + e0 + 64 * wi) * 2u, 4096);
    }
  }
}

__global__ __launch_bounds__(768, 3) void fused_mha_fnet_kernel(Args a) {
  extern __shared__ __attribute__((aligned(16))) char lds[];
  Ids id; id.lane = threadIdx.x & 63; id.wid = __builtin_amdgcn_readfirstlane(threadIdx.x >> 6);
  id.xcd = blockIdx.x & 7; id.idx = blockIdx.x >> 3;
  id.lds0 = (unsigned)(size_t)(LAS char*)lds;
  if (id.wid < 4) role_g0(a, id);
  else if (id.wid < 8) role_g1(a, id);
  else role_dma(a, id);
}

extern "C" void kernel_launch(void* const* d_in, const int* in_sizes, int n_in, void* d_out, int out_size, void* d_ws, size_t ws_size, hipStream_t stream) {
  u16* ws = (u16*)d_ws; const size_t M = (size_t)2048 * 1024;
  Args a;
  a.Ax = (const u16*)d_in[0]; a.wqkv = (const u16*)d_in[1]; a.wo = (const u16*)d_in[2]; a.Bx = (const u16*)d_in[3];
  a.Cr = (const u16*)d_in[4]; a.Ci = (const u16*)d_in[5]; a.Wr = (const u16*)d_in[6]; a.Wi = (const u16*)d_in[7];
  a.Q = ws; a.ABt = ws + 3 * M; a.O = ws + 5 * M; a.X = ws + 6 * M; a.out = (u16*)d_out; a.flg = (u64*)(ws + 8 * M);
  (void)hipFuncSetAttribute((const void*)fused_mha_fnet_kernel, hipFuncAttributeMaxDynamicSharedMemorySize, LDS_ALL);
  fused_mha_fnet_kernel<<<256, 768, LDS_ALL, stream>>>(a);
}
```
